```python
import torch

def _softmax(x, axis=-1):
    return torch.softmax(x, dim=axis)
def _gelu(x):
    return torch.nn.functional.gelu(x, approximate="tanh")
def _silu(x):
    return torch.nn.functional.silu(x)
def _sigmoid(x):
    return torch.sigmoid(x)
def _layernorm(x, axis=-1, eps=1e-5):
    m = x.mean(dim=axis, keepdim=True)
    v = x.var(dim=axis, keepdim=True, unbiased=False)
    return (x - m) / torch.sqrt(v + eps)
def _scatter_add(tgt, idx, src, axis=0):
    return tgt.clone().index_add_(axis, idx, src)
def _segment_sum(src, seg, n):
    out = torch.zeros((n,) + src.shape[1:], dtype=src.dtype)
    return out.index_add_(0, seg, src)
def _pad(x, pw):
    flat = tuple(v for pair in reversed(pw) for v in pair)
    return torch.nn.functional.pad(x, flat)

kernel_name = 'interleave_layers__sliding_block__glu_gated_swiglu__prompt'

def setup_inputs():

    _w = (lambda a: (lambda t: t.to(torch.bfloat16) if t.is_floating_point() else t)(torch.as_tensor(a)))
    def _A_setup():
        B, S, D = 1, 2048, 448
        return {
            "x": _w((lambda *s: torch.randn(*s, dtype=torch.bfloat16) * 0.05)(B, S, D)),
            "wqkv": _w((lambda *s: torch.randn(*s, dtype=torch.bfloat16) * 0.05)(D, 3 * D)),
            "wo": _w((lambda *s: torch.randn(*s, dtype=torch.bfloat16) * 0.05)(D, D)),
        }
    _A_in = _A_setup()

    def _B_setup():
        B, S, D, F = 1, 2048, 448, 1792
        return {
            "x": _w((lambda *s: torch.randn(*s, dtype=torch.bfloat16) * 0.05)(B, S, D)),
            "w1": _w((lambda *s: torch.randn(*s, dtype=torch.bfloat16) * 0.8)(D, F) * (2.0 / D) ** 0.5),
            "w3": _w((lambda *s: torch.randn(*s, dtype=torch.bfloat16) * 0.8)(D, F) * (2.0 / D) ** 0.5),
            "w2": _w((lambda *s: torch.randn(*s, dtype=torch.bfloat16) * 0.8)(F, D) * (2.0 / F) ** 0.5),
        }
    _B_in = _B_setup()

    return {
        "A_x": _A_in["x"],
        "A_wqkv": _A_in["wqkv"],
        "A_wo": _A_in["wo"],
        "B_x": _B_in["x"],
        "B_w1": _B_in["w1"],
        "B_w3": _B_in["w3"],
        "B_w2": _B_in["w2"],
    }

def reference(A_x, A_wqkv, A_wo, B_x, B_w1, B_w3, B_w2):
    def _A_ref(x, wqkv, wo):
        B, S, D = x.shape
        H = 16; Dh = D // H; W = 256
        qkv = (lambda x, *d: x.permute(*d))((x @ wqkv).reshape(B, S, 3, H, Dh), 2, 0, 3, 1, 4)
        q, k, v = qkv[0], qkv[1], qkv[2]
        scores = torch.einsum("bhqd,bhkd->bhqk", q, k) * (Dh ** -0.5)
        idx = torch.arange(S)
        mask = (idx[:, None] - idx[None, :] >= 0) & (idx[:, None] - idx[None, :] < W)
        scores = torch.where(mask[None, None], scores, -1e9)
        p = _softmax(scores, axis=-1)
        o = torch.einsum("bhqk,bhkd->bhqd", p, v)
        return (lambda x, *d: x.permute(*d))(o, 0, 2, 1, 3).reshape(B, S, D) @ wo

    def _B_ref(x, w1, w3, w2):
        gate = torch.einsum("bsd,df->bsf", x, w1)
        up = torch.einsum("bsd,df->bsf", x, w3)
        h = _silu(gate) * up
        out = torch.einsum("bsf,fd->bsd", h, w2)
        return out

    x = A_x
    x = x + _A_ref(_layernorm(x, axis=-1), A_wqkv, A_wo)
    x = x + _B_ref(_layernorm(x, axis=-1), B_w1, B_w3, B_w2)
    x = x + _A_ref(_layernorm(x, axis=-1), A_wqkv, A_wo)
    x = x + _B_ref(_layernorm(x, axis=-1), B_w1, B_w3, B_w2)
    return x
```

```cpp
#include <hip/hip_runtime.h>
#include <hip/hip_bf16.h>
#include <stdint.h>
typedef unsigned short u16;
__device__ __forceinline__ float b2f(u16 v){ return __uint_as_float(((unsigned)v)<<16); }
__device__ __forceinline__ u16 f2b(float f){ unsigned u=__float_as_uint(f); u += 0x7fffu + ((u>>16)&1u); return (u16)(u>>16); }
__device__ __forceinline__ float rb(float f){ return b2f(f2b(f)); }
constexpr int S=2048, D=448, H=16, DH=28, W=256, F=1792;

__global__ void n_ln(const u16* x, u16* y){
  int row=blockIdx.x; __shared__ float red[64];
  int t=threadIdx.x;
  float s=0; for(int i=t;i<D;i+=64) s+=b2f(x[row*D+i]);
  for(int o=32;o;o>>=1) s+=__shfl_xor(s,o);
  float m=rb(s/D);
  float mf=s/D;
  float q=0; for(int i=t;i<D;i+=64){ float d=b2f(x[row*D+i])-mf; q+=d*d; }
  for(int o=32;o;o>>=1) q+=__shfl_xor(q,o);
  float v=rb(q/D);
  float sd=rb(sqrtf(rb(v+1e-5f)));
  for(int i=t;i<D;i+=64){ float d=rb(b2f(x[row*D+i])-m); y[row*D+i]=f2b(d/sd); }
}
__global__ void n_gemm(const u16* a, const u16* b, const u16* res, u16* out, int M,int N,int K){
  int n=blockIdx.x*blockDim.x+threadIdx.x; int m=blockIdx.y; if(n>=N) return;
  float acc=0; for(int k=0;k<K;k++) acc+=b2f(a[m*K+k])*b2f(b[k*N+n]);
  float r=rb(acc); if(res) r=rb(r+b2f(res[m*N+n]));
  out[m*N+n]=f2b(r);
}
__global__ void n_attn(const u16* qkv, u16* o){
  int q=blockIdx.x, h=blockIdx.y; int t=threadIdx.x;
  __shared__ float sc[256]; __shared__ float red[256];
  int key=q-t; float s=-1e30f;
  if(key>=0){ float acc=0; for(int d=0;d<DH;d++) acc+=b2f(qkv[q*3*D+h*DH+d])*b2f(qkv[key*3*D+D+h*DH+d]); s=rb(rb(acc)*rb(0.18898223650461362f)); }
  sc[t]=s; red[t]=s; __syncthreads();
  for(int st=128;st;st>>=1){ if(t<st) red[t]=fmaxf(red[t],red[t+st]); __syncthreads(); }
  float mx=red[0]; __syncthreads();
  float e=(key>=0)?expf(s-mx):0.f; red[t]=e; __syncthreads();
  for(int st=128;st;st>>=1){ if(t<st) red[t]+=red[t+st]; __syncthreads(); }
  float sum=red[0]; sc[t]=rb(e/sum); __syncthreads();
  if(t<DH){ float acc=0; for(int j=0;j<256;j++){ int k2=q-j; if(k2>=0) acc+=sc[j]*b2f(qkv[k2*3*D+2*D+h*DH+t]); } o[q*D+h*DH+t]=f2b(acc); }
}
__global__ void n_swiglu(const u16* g,const u16* u,u16* h,int n){
  int i=blockIdx.x*blockDim.x+threadIdx.x; if(i>=n) return;
  float a=b2f(g[i]); float s=rb(a/(1.f+expf(-a))); h[i]=f2b(s*b2f(u[i]));
}
extern "C" void kernel_launch(void* const* d_in, const int* in_sizes, int n_in,
                              void* d_out, int out_size, void* d_ws, size_t ws_size,
                              hipStream_t stream) {
  const u16* x=(const u16*)d_in[0]; const u16* wqkv=(const u16*)d_in[1]; const u16* wo=(const u16*)d_in[2];
  const u16* w1=(const u16*)d_in[4]; const u16* w3=(const u16*)d_in[5]; const u16* w2=(const u16*)d_in[6];
  u16* ws=(u16*)d_ws;
  u16* ln=ws; u16* qkv=ln+S*D; u16* o=qkv+S*3*D; u16* x1=o+S*D; u16* g=x1+S*D; u16* u=g+S*F; u16* h=u+S*F; u16* x2=h+S*F;
  const u16* cur=x;
  for(int l=0;l<2;l++){
    n_ln<<<S,64,0,stream>>>(cur,ln);
    n_gemm<<<dim3((3*D+255)/256,S),256,0,stream>>>(ln,wqkv,nullptr,qkv,S,3*D,D);
    n_attn<<<dim3(S,H),256,0,stream>>>(qkv,o);
    n_gemm<<<dim3(2,S),256,0,stream>>>(o,wo,cur,x1,S,D,D);
    n_ln<<<S,64,0,stream>>>(x1,ln);
    n_gemm<<<dim3(F/256,S),256,0,stream>>>(ln,w1,nullptr,g,S,F,D);
    n_gemm<<<dim3(F/256,S),256,0,stream>>>(ln,w3,nullptr,u,S,F,D);
    n_swiglu<<<S*F/256,256,0,stream>>>(g,u,h,S*F);
    u16* dst=(l==0)?x2:(u16*)d_out;
    n_gemm<<<dim3(2,S),256,0,stream>>>(h,w2,x1,dst,S,D,F);
    cur=x2;
  }
}
```

```cpp
#include <hip/hip_runtime.h>
#include <stdint.h>

typedef unsigned short u16;
typedef __attribute__((ext_vector_type(8))) __bf16 bf16x8;
typedef __attribute__((ext_vector_type(2))) __bf16 bf16x2;
typedef __attribute__((ext_vector_type(4))) short s16x4;
typedef __attribute__((ext_vector_type(16))) float f32x16;
typedef __attribute__((ext_vector_type(4))) float f32x4;
typedef __attribute__((ext_vector_type(2))) float f32x2;
typedef __attribute__((ext_vector_type(4))) unsigned u32x4;
typedef __attribute__((ext_vector_type(2))) unsigned u32x2;
#define LAS __attribute__((address_space(3)))
#define DI __device__ __forceinline__
#define MFMA(a, b, c) __builtin_amdgcn_mfma_f32_32x32x16_bf16((a), (b), (c), 0, 0, 0)

constexpr int S = 2048, D = 448, NH = 16, F = 1792;
constexpr int NTT = S / 32;
constexpr int KS_D = D / 16;
constexpr int KS_F = F / 16;
constexpr int KS_O = NH * 2;
constexpr float LN_EPS = 1e-5f;
constexpr float QSCALE = 0.18898223650461362f * 1.4426950408889634f;
constexpr float LOG2E = 1.4426950408889634f;

constexpr size_t WS_X1 = 0;
constexpr size_t WS_X2 = WS_X1 + (size_t)NTT * KS_D * 1024;
constexpr size_t WS_Q = WS_X2 + (size_t)NTT * KS_D * 1024;
constexpr size_t WS_K = WS_Q + (size_t)NH * NTT * 2048;
constexpr size_t WS_V = WS_K + (size_t)NH * NTT * 2048;
constexpr size_t WS_O = WS_V + (size_t)NH * NTT * 2048;
constexpr size_t WS_H = WS_O + (size_t)NTT * KS_O * 1024;
constexpr size_t WS_ST1 = WS_H + (size_t)NTT * KS_F * 1024;
constexpr size_t WS_ST2 = WS_ST1 + (size_t)NTT * 14 * 32 * 8;
constexpr size_t WS_END = WS_ST2 + (size_t)NTT * 14 * 32 * 8;

DI bf16x8 as_bf(u32x4 v) { return __builtin_bit_cast(bf16x8, v); }
DI unsigned pk2(float a, float b) { bf16x2 p = {(__bf16)a, (__bf16)b}; return __builtin_bit_cast(unsigned, p); }
DI float bf_lo(unsigned v) { return __uint_as_float(v << 16); }
DI float bf_hi(unsigned v) { return __uint_as_float(v & 0xffff0000u); }
DI float rbf(float a) { return (float)(__bf16)a; }
DI float dot2(unsigned a, unsigned b, float c) {
  return __builtin_amdgcn_fdot2_f32_bf16(__builtin_bit_cast(bf16x2, a), __builtin_bit_cast(bf16x2, b), c, false);
}
DI float xhalf_sum(float x) {
  auto r = __builtin_amdgcn_permlane32_swap(__float_as_uint(x), __float_as_uint(x), false, false);
  return __uint_as_float(r[0]) + __uint_as_float(r[1]);
}
DI float xhalf_max(float x) {
  auto r = __builtin_amdgcn_permlane32_swap(__float_as_uint(x), __float_as_uint(x), false, false);
  return fmaxf(__uint_as_float(r[0]), __uint_as_float(r[1]));
}
DI bf16x8 ld_tr(LAS char* p, int off0, int off1) {
  s16x4 a = __builtin_amdgcn_ds_read_tr16_b64_v4i16((LAS s16x4*)(p + off0));
  s16x4 b = __builtin_amdgcn_ds_read_tr16_b64_v4i16((LAS s16x4*)(p + off1));
  return __builtin_bit_cast(bf16x8, __builtin_shufflevector(a, b, 0, 1, 2, 3, 4, 5, 6, 7));
}
DI void dma1k(__amdgpu_buffer_rsrc_t rsrc, unsigned voff, unsigned soff, unsigned lds_addr) {
  unsigned keep;
  asm volatile("s_mov_b32 %0, m0\n\ts_mov_b32 m0, %4\n\ts_nop 0\n\tbuffer_load_dwordx4 %1, %2, %3 offen lds\n\ts_mov_b32 m0, %0"
               : "=&s"(keep) : "v"(voff), "s"(rsrc), "s"(soff), "s"(lds_addr) : "memory");
}
#define BARRIER() asm volatile("s_waitcnt lgkmcnt(0)\n\ts_barrier" ::: "memory")
DI __amdgpu_buffer_rsrc_t mkrsrc(const void* p, unsigned bytes) {
  return __builtin_amdgcn_make_buffer_rsrc((void*)p, (short)0, (int)bytes, 0x00020000);
}
DI int wstart(int head) { return 56 * (head >> 1) + 24 * (head & 1); }
DI u32x4 pack8(const f32x16& a, int s) {
  u32x4 r;
  r[0] = pk2(a[8 * s + 0], a[8 * s + 1]); r[1] = pk2(a[8 * s + 2], a[8 * s + 3]);
  r[2] = pk2(a[8 * s + 4], a[8 * s + 5]); r[3] = pk2(a[8 * s + 6], a[8 * s + 7]);
  return r;
}
DI u32x4 corr_frag_mean(float mean, int h) {
  float hi = rbf(-mean), lo = rbf(-mean - hi);
  unsigned p = pk2(hi, lo);
  u32x4 r; r[0] = h ? 0u : p; r[1] = h ? 0u : p; r[2] = 0u; r[3] = 0u;
  return r;
}
DI u32x4 corr_frag_cs(float cs, int h) {
  float hi = rbf(cs), lo = rbf(cs - hi);
  u32x4 r; r[0] = h ? 0u : pk2(hi, hi); r[1] = h ? 0u : pk2(lo, lo); r[2] = 0u; r[3] = 0u;
  return r;
}

constexpr int K1_LDS = 3 * 28672 + 4 * 3 * 32 * 4 + 4 * 32 * 4;
template <bool RAWX>
__global__ __launch_bounds__(256) void k_qkv(const char* __restrict__ x, const f32x2* __restrict__ stats,
                                             const u16* __restrict__ wqkv, char* __restrict__ ws) {
  extern __shared__ __attribute__((aligned(16))) char smem[];
  LAS char* sm = (LAS char*)smem;
  const int b = blockIdx.x, xcd = b & 7, idx = b >> 3;
  const int grp = xcd * 2 + (idx & 1), head = idx >> 1;
  const int tid = threadIdx.x, lane = tid & 63, w = __builtin_amdgcn_readfirstlane(tid >> 6);
  const int t = lane & 31, h = lane >> 5;
  const int T = grp * 4 + w;
  const unsigned ldsbase = (unsigned)(size_t)smem;
  {
    __amdgpu_buffer_rsrc_t rs = mkrsrc(wqkv, D * 3 * D * 2);
    unsigned voff = (lane >> 2) * 2688 + (lane & 3) * 16 + wstart(head) * 2;
#pragma unroll
    for (int i = 0; i < 7; ++i)
#pragma unroll
      for (int m = 0; m < 3; ++m) {
        int ii = i * 4 + w;
        dma1k(rs, voff, ii * 16 * 2688 + m * 896, ldsbase + m * 28672 + ii * 1024);
      }
  }
  u32x4 xf[KS_D];
#pragma unroll
  for (int i = 0; i < KS_D; ++i) {
    int s = (i & ~3) + (((i & 3) + w) & 3);
    if (RAWX) xf[i] = *(const u32x4*)(x + ((size_t)(T * 32 + t) * D + s * 16 + h * 8) * 2);
    else      xf[i] = *(const u32x4*)(x + ((size_t)(T * KS_D + s) * 64 + lane) * 16);
  }
  float sum = 0.f, sq = 0.f;
  if (!RAWX) {
#pragma unroll
    for (int i = 0; i < 7; ++i) { f32x2 st = stats[(T * 14 + h * 7 + i) * 32 + t]; sum += st[0]; sq += st[1]; }
  }
  asm volatile("" ::"v"(xf[0]) : "memory");
  BARRIER();
  const int blk = (lane >> 4) & 1, q = (lane & 15) >> 2, p = lane & 3;
  constexpr int STEP = RAWX ? 4 : 8;
  const int rowoff = RAWX ? 8 * h : 4 * h;
  LAS char* wb[4];
#pragma unroll
  for (int j = 0; j < 4; ++j) wb[j] = sm + (rowoff + q) * 64 + blk * 32 + p * 8 + ((j + w) & 3) * 1024;
  f32x16 aq = {}, ak = {}, av = {};
  float csq = 0.f, csk = 0.f, csv = 0.f;
#pragma unroll
  for (int i = 0; i < KS_D; ++i) {
    const int o = (i & ~3) * 1024;
    bf16x8 wq = ld_tr(wb[i & 3], o, o + STEP * 64);
    bf16x8 wk = ld_tr(wb[i & 3], o + 28672, o + 28672 + STEP * 64);
    bf16x8 wv = ld_tr(wb[i & 3], o + 2 * 28672, o + 2 * 28672 + STEP * 64);
    bf16x8 xb = as_bf(xf[i]);
    aq = MFMA(wq, xb, aq);
    ak = MFMA(wk, xb, ak);
    av = MFMA(xb, wv, av);
    if (RAWX) {
#pragma unroll
      for (int j = 0; j < 4; ++j) { sum = dot2(xf[i][j], 0x3f803f80u, sum); sq = dot2(xf[i][j], xf[i][j], sq); }
    }
    if ((i & 3) == 0) {
      u32x4 a = __builtin_bit_cast(u32x4, wq), c = __builtin_bit_cast(u32x4, wk), e = __builtin_bit_cast(u32x4, wv);
#pragma unroll
      for (int j = 0; j < 4; ++j) { csq = dot2(a[j], 0x3f803f80u, csq); csk = dot2(c[j], 0x3f803f80u, csk); csv = dot2(e[j], 0x3f803f80u, csv); }
    }
  }
  sum = xhalf_sum(sum); sq = xhalf_sum(sq);
  const float mean = sum * (1.f / D);
  const float var = fmaxf(sq * (1.f / D) - mean * mean, 0.f);
  const float rstd = __builtin_amdgcn_rsqf(var + LN_EPS);
  csq = xhalf_sum(csq); csk = xhalf_sum(csk); csv = xhalf_sum(csv);
  LAS float* csp = (LAS float*)(sm + 3 * 28672);
  LAS float* rsl = csp + 4 * 3 * 32;
  if (h == 0) { csp[(w * 3 + 0) * 32 + t] = csq; csp[(w * 3 + 1) * 32 + t] = csk; csp[(w * 3 + 2) * 32 + t] = csv; rsl[w * 32 + t] = rstd; }
  BARRIER();
  float c0 = 0.f, c1 = 0.f, c2 = 0.f;
#pragma unroll
  for (int j = 0; j < 4; ++j) { c0 += csp[(j * 3 + 0) * 32 + t]; c1 += csp[(j * 3 + 1) * 32 + t]; c2 += csp[(j * 3 + 2) * 32 + t]; }
  bf16x8 mf = as_bf(corr_frag_mean(mean, h));
  aq = MFMA(as_bf(corr_frag_cs(c0, h)), mf, aq);
  ak = MFMA(as_bf(corr_frag_cs(c1, h)), mf, ak);
  av = MFMA(mf, as_bf(corr_frag_cs(c2, h)), av);
  const bool odd = head & 1;
  {
    const float sc = rstd * QSCALE;
#pragma unroll
    for (int r = 0; r < 16; ++r) aq[r] *= sc;
    const bool padlo = odd && h == 0, padhi = !odd && h == 1;
#pragma unroll
    for (int r = 0; r < 4; ++r) { aq[r] = padlo ? 0.f : aq[r]; aq[12 + r] = padhi ? 0.f : aq[12 + r]; }
#pragma unroll
    for (int r = 0; r < 16; ++r) ak[r] *= rstd;
#pragma unroll
    for (int g = 0; g < 4; ++g) {
      f32x4 rr = *(LAS f32x4*)(rsl + w * 32 + 8 * g + 4 * h);
#pragma unroll
      for (int r = 0; r < 4; ++r) av[4 * g + r] *= rr[r];
    }
    const bool one = t == (odd ? 0 : 28);
#pragma unroll
    for (int r = 0; r < 16; ++r) av[r] = one ? 1.f : av[r];
  }
  const size_t fo = ((size_t)(head * NTT + T) * 2) * 1024 + lane * 16;
#pragma unroll
  for (int s = 0; s < 2; ++s) {
    *(u32x4*)(ws + WS_Q + fo + s * 1024) = pack8(aq, s);
    *(u32x4*)(ws + WS_K + fo + s * 1024) = pack8(ak, s);
    *(u32x4*)(ws + WS_V + fo + s * 1024) = pack8(av, s);
  }
}

__global__ __launch_bounds__(256) void k_attn(char* __restrict__ ws) {
  const int b = blockIdx.x, xcd = b & 7, idx = b >> 3;
  const int grp = xcd * 2 + (idx & 1), head = idx >> 1;
  const int tid = threadIdx.x, lane = tid & 63, w = __builtin_amdgcn_readfirstlane(tid >> 6);
  const int c = lane & 31, h = lane >> 5;
  const int T = grp * 4 + w;
  const char* qb = ws + WS_Q + (size_t)head * NTT * 2048 + lane * 16;
  const char* kb = ws + WS_K + (size_t)head * NTT * 2048 + lane * 16;
  const char* vb = ws + WS_V + (size_t)head * NTT * 2048 + lane * 16;
  u32x4 qf[2], kf[9][2], vf[9][2];
#pragma unroll
  for (int s = 0; s < 2; ++s) qf[s] = *(const u32x4*)(qb + (size_t)T * 2048 + s * 1024);
#pragma unroll
  for (int j = 0; j < 9; ++j) {
    int kt = T - 8 + j; kt = kt < 0 ? 0 : kt;
#pragma unroll
    for (int s = 0; s < 2; ++s) kf[j][s] = *(const u32x4*)(kb + (size_t)kt * 2048 + s * 1024);
  }
#pragma unroll
  for (int j = 0; j < 9; ++j) {
    int kt = T - 8 + j; kt = kt < 0 ? 0 : kt;
#pragma unroll
    for (int s = 0; s < 2; ++s) vf[j][s] = *(const u32x4*)(vb + (size_t)kt * 2048 + s * 1024);
  }
  f32x16 sc[9];
#pragma unroll
  for (int j = 0; j < 9; ++j) {
    f32x16 z = {};
    z = MFMA(as_bf(kf[j][0]), as_bf(qf[0]), z);
    sc[j] = MFMA(as_bf(kf[j][1]), as_bf(qf[1]), z);
  }
  constexpr float NEG = -1e30f;
  const int vv = c - 4 * h;
#pragma unroll
  for (int r = 0; r < 16; ++r) {
    const bool le = ((r & 3) + 8 * (r >> 2)) <= vv;
    sc[8][r] = le ? sc[8][r] : NEG;
    sc[0][r] = le ? NEG : sc[0][r];
  }
#pragma unroll
  for (int j = 0; j < 8; ++j)
    if (T - 8 + j < 0) {
#pragma unroll
      for (int r = 0; r < 16; ++r) sc[j][r] = NEG;
    }
  float m = NEG;
#pragma unroll
  for (int j = 0; j < 9; ++j)
#pragma unroll
    for (int r = 0; r < 16; r += 2) m = fmaxf(fmaxf(m, sc[j][r]), sc[j][r + 1]);
  m = xhalf_max(m);
  f32x16 o = {};
#pragma unroll
  for (int j = 0; j < 9; ++j) {
#pragma unroll
    for (int r = 0; r < 16; ++r) sc[j][r] = __builtin_amdgcn_exp2f(sc[j][r] - m);
#pragma unroll
    for (int s = 0; s < 2; ++s) o = MFMA(as_bf(vf[j][s]), as_bf(pack8(sc[j], s)), o);
  }
  const bool odd = head & 1;
  float l = odd ? o[0] : o[12];
  {
    auto r = __builtin_amdgcn_permlane32_swap(__float_as_uint(l), __float_as_uint(l), false, false);
    l = __uint_as_float(odd ? r[0] : r[1]);
  }
  const float inv = __builtin_amdgcn_rcpf(l);
#pragma unroll
  for (int r = 0; r < 16; ++r) o[r] *= inv;
  const bool padlo = odd && h == 0, padhi = !odd && h == 1;
#pragma unroll
  for (int r = 0; r < 4; ++r) { o[r] = padlo ? 0.f : o[r]; o[12 + r] = padhi ? 0.f : o[12 + r]; }
  char* ob = ws + WS_O + ((size_t)(T * KS_O + head * 2) * 64 + lane) * 16;
#pragma unroll
  for (int s = 0; s < 2; ++s) *(u32x4*)(ob + s * 1024) = pack8(o, s);
}

template <bool RES_RAW, bool OUT_RAW>
DI void res_epilogue(f32x16 acc, const u32x4 (&rf)[2], const u32x2 (&rr)[4], int T, int dt  ,
                     int lane, char* outp, f32x2* stats) {
  const int t = lane & 31, h = lane >> 5;
  if (RES_RAW) {
#pragma unroll
    for (int g = 0; g < 4; ++g) {
      acc[4 * g + 0] += bf_lo(rr[g][0]); acc[4 * g + 1] += bf_hi(rr[g][0]);
      acc[4 * g + 2] += bf_lo(rr[g][1]); acc[4 * g + 3] += bf_hi(rr[g][1]);
    }
  } else {
#pragma unroll
    for (int s = 0; s < 2; ++s)
#pragma unroll
      for (int j = 0; j < 4; ++j) { acc[8 * s + 2 * j] += bf_lo(rf[s][j]); acc[8 * s + 2 * j + 1] += bf_hi(rf[s][j]); }
  }
  u32x4 o0 = pack8(acc, 0), o1 = pack8(acc, 1);
  if (OUT_RAW) {
    char* rowp = outp + ((size_t)(T * 32 + t) * D + dt * 32 + 4 * h) * 2;
    *(u32x2*)(rowp + 0) = u32x2{o0[0], o0[1]};
    *(u32x2*)(rowp + 16) = u32x2{o0[2], o0[3]};
    *(u32x2*)(rowp + 32) = u32x2{o1[0], o1[1]};
    *(u32x2*)(rowp + 48) = u32x2{o1[2], o1[3]};
  } else {
    char* fp = outp + ((size_t)(T * KS_D + dt * 2) * 64 + lane) * 16;
    *(u32x4*)(fp) = o0;
    *(u32x4*)(fp + 1024) = o1;
    float sum = 0.f, sq = 0.f;
#pragma unroll
    for (int j = 0; j < 4; ++j) {
      float a = bf_lo(o0[j]), c = bf_hi(o0[j]), e = bf_lo(o1[j]), f = bf_hi(o1[j]);
      sum += (a + c) + (e + f);
      sq = fmaf(a, a, sq); sq = fmaf(c, c, sq); sq = fmaf(e, e, sq); sq = fmaf(f, f, sq);
    }
    sum = xhalf_sum(sum); sq = xhalf_sum(sq);
    if (h == 0) stats[(T * 14 + dt) * 32 + t] = f32x2{sum, sq};
  }
}

DI unsigned dma_voff_128(int lane, int rowpitch, int m) {
  const int row = lane >> 3, cp = lane & 7;
  const int seg = (cp >> 1) ^ (((row >> 1) & 1) << 1) ^ m;
  return row * rowpitch + (seg * 2 + (cp & 1)) * 16;
}

constexpr int K3_LDS = 448 * 128;
template <bool RES_RAW>
__global__ __launch_bounds__(256) void k_wo(const char* __restrict__ xres, const u16* __restrict__ wo, char* __restrict__ ws) {
  extern __shared__ __attribute__((aligned(16))) char smem[];
  LAS char* sm = (LAS char*)smem;
  const int b = blockIdx.x, xcd = b & 7, idx = b >> 3;
  const int tp = xcd * 4 + (idx & 3), ns = idx >> 2;
  const int tid = threadIdx.x, lane = tid & 63, w = __builtin_amdgcn_readfirstlane(tid >> 6);
  const int t = lane & 31, h = lane >> 5;
  const int tt = w & 1, kh = w >> 1, T = tp * 2 + tt;
  const unsigned ldsbase = (unsigned)(size_t)smem;
  {
    __amdgpu_buffer_rsrc_t rs = mkrsrc(wo, D * D * 2);
    unsigned voff = dma_voff_128(lane, 896, 0);
#pragma unroll
    for (int i = 0; i < 14; ++i) { int ii = i * 4 + w; dma1k(rs, voff, ii * 8 * 896 + ns * 128, ldsbase + ii * 1024); }
  }
  u32x4 of[16];
#pragma unroll
  for (int i = 0; i < 16; ++i) of[i] = *(const u32x4*)(ws + WS_O + ((size_t)(T * KS_O + kh * 16 + i) * 64 + lane) * 16);
  u32x4 rf[2]; u32x2 rr[4];
  const int dt = ns * 2 + kh;
  if (RES_RAW) {
#pragma unroll
    for (int g = 0; g < 4; ++g) rr[g] = *(const u32x2*)(xres + ((size_t)(T * 32 + t) * D + dt * 32 + 8 * g + 4 * h) * 2);
  } else {
#pragma unroll
    for (int s = 0; s < 2; ++s) rf[s] = *(const u32x4*)(xres + ((size_t)(T * KS_D + dt * 2 + s) * 64 + lane) * 16);
  }
  asm volatile("" ::"v"(of[0]) : "memory");
  BARRIER();
  const int blk = (lane >> 4) & 1, q = (lane & 15) >> 2, p = lane & 3;
  LAS char* wb[2];
#pragma unroll
  for (int d2 = 0; d2 < 2; ++d2)
    wb[d2] = sm + (4 * h + q + kh * 224) * 128 + (((2 * d2 + blk) ^ ((q >> 1) << 1)) << 5) + p * 8;
  f32x16 acc[2] = {};
#pragma unroll
  for (int i = 0; i < 16; ++i) {
    const int hd = i >> 1, s = i & 1;
    const int row = 56 * (hd >> 1) + 24 * (hd & 1) + 16 * s;
#pragma unroll
    for (int d2 = 0; d2 < 2; ++d2) acc[d2] = MFMA(ld_tr(wb[d2], row * 128, (row + 8) * 128), as_bf(of[i]), acc[d2]);
  }
  BARRIER();
  LAS f32x4* ex = (LAS f32x4*)(sm) + (size_t)(tt * 2 + kh) * 256;
  {
    const f32x16& give = kh ? acc[0] : acc[1];
#pragma unroll
    for (int g = 0; g < 4; ++g) ex[g * 64 + lane] = f32x4{give[4 * g], give[4 * g + 1], give[4 * g + 2], give[4 * g + 3]};
  }
  BARRIER();
  f32x16 keep = kh ? acc[1] : acc[0];
  {
    LAS f32x4* rd = (LAS f32x4*)(sm) + (size_t)(tt * 2 + (kh ^ 1)) * 256;
#pragma unroll
    for (int g = 0; g < 4; ++g) { f32x4 v = rd[g * 64 + lane]; keep[4 * g] += v[0]; keep[4 * g + 1] += v[1]; keep[4 * g + 2] += v[2]; keep[4 * g + 3] += v[3]; }
  }
  res_epilogue<RES_RAW, false>(keep, rf, rr, T, dt, lane, ws + WS_X1, (f32x2*)(ws + WS_ST1));
}

constexpr int K4_LDS = 2 * 57344 + 4 * 4 * 32 * 4;
__global__ __launch_bounds__(512) void k_ffn1(const u16* __restrict__ w1, const u16* __restrict__ w3, char* __restrict__ ws) {
  extern __shared__ __attribute__((aligned(16))) char smem[];
  LAS char* sm = (LAS char*)smem;
  const int b = blockIdx.x, xcd = b & 7, fs = b >> 3;
  const int tid = threadIdx.x, lane = tid & 63, w = __builtin_amdgcn_readfirstlane(tid >> 6);
  const int t = lane & 31, h = lane >> 5;
  const int T = xcd * 8 + w;
  const unsigned ldsbase = (unsigned)(size_t)smem;
  {
    __amdgpu_buffer_rsrc_t r1 = mkrsrc(w1, D * F * 2), r3 = mkrsrc(w3, D * F * 2);
    unsigned v1 = dma_voff_128(lane, 3584, 0), v3 = dma_voff_128(lane, 3584, 1);
#pragma unroll
    for (int i = 0; i < 7; ++i) {
      int ii = i * 8 + w;
      dma1k(r1, v1, ii * 8 * 3584 + fs * 128, ldsbase + ii * 1024);
      dma1k(r3, v3, ii * 8 * 3584 + fs * 128, ldsbase + 57344 + ii * 1024);
    }
  }
  const int w4 = w & 3;
  u32x4 xf[KS_D];
#pragma unroll
  for (int i = 0; i < KS_D; ++i) {
    int s = (i & ~3) + (((i & 3) + w4) & 3);
    xf[i] = *(const u32x4*)(ws + WS_X1 + ((size_t)(T * KS_D + s) * 64 + lane) * 16);
  }
  float sum = 0.f, sq = 0.f;
  {
    const f32x2* stats = (const f32x2*)(ws + WS_ST1);
#pragma unroll
    for (int i = 0; i < 7; ++i) { f32x2 st = stats[(T * 14 + h * 7 + i) * 32 + t]; sum += st[0]; sq += st[1]; }
  }
  asm volatile("" ::"v"(xf[0]) : "memory");
  BARRIER();
  const int blk = (lane >> 4) & 1, q = (lane & 15) >> 2, p = lane & 3;
  f32x16 acc[4] = {};
  float cs[4] = {0.f, 0.f, 0.f, 0.f};
  const int lanebase = blk * 57344 + (4 * h + q) * 128 + p * 8;
  const int xr = ((q >> 1) << 1) ^ blk;
#pragma unroll
  for (int i = 0; i < KS_D; ++i) {
    LAS char* base = sm + lanebase + (((i & 3) + w4) & 3) * 2048 + (i & ~3) * 2048;
#pragma unroll
    for (int f = 0; f < 4; ++f) {
      bf16x8 a = ld_tr(base + ((f ^ xr) << 5), 0, 8 * 128);
      acc[f] = MFMA(a, as_bf(xf[i]), acc[f]);
      if ((i & 3) == 0) {
        u32x4 u = __builtin_bit_cast(u32x4, a);
#pragma unroll
        for (int j = 0; j < 4; ++j) cs[f] = dot2(u[j], 0x3f803f80u, cs[f]);
      }
    }
  }
  sum = xhalf_sum(sum); sq = xhalf_sum(sq);
  const float mean = sum * (1.f / D);
  const float var = fmaxf(sq * (1.f / D) - mean * mean, 0.f) + LN_EPS;
  const float rstd = __builtin_amdgcn_rsqf(var);
  LAS float* csp = (LAS float*)(sm + 2 * 57344);
#pragma unroll
  for (int f = 0; f < 4; ++f) { cs[f] = xhalf_sum(cs[f]); if (h == 0 && w < 4) csp[(w * 4 + f) * 32 + t] = cs[f]; }
  BARRIER();
  bf16x8 mf = as_bf(corr_frag_mean(mean, h));
  const float c1 = -rstd * LOG2E;
#pragma unroll
  for (int f = 0; f < 4; ++f) {
    float c = 0.f;
#pragma unroll
    for (int j = 0; j < 4; ++j) c += csp[(j * 4 + f) * 32 + t];
    acc[f] = MFMA(as_bf(corr_frag_cs(c, h)), mf, acc[f]);
  }
#pragma unroll
  for (int f = 0; f < 4; ++f) {
    float hv[8];
#pragma unroll
    for (int r = 0; r < 8; ++r) {
      const float g = acc[f][r], u = acc[f][8 + r];
      const float e = __builtin_amdgcn_exp2f(g * c1);
      const float rc = __builtin_amdgcn_rcpf(fmaf(e, var, var));
      hv[r] = (g * u) * rc;
    }
    u32x4 o = {pk2(hv[0], hv[1]), pk2(hv[2], hv[3]), pk2(hv[4], hv[5]), pk2(hv[6], hv[7])};
    *(u32x4*)(ws + WS_H + ((size_t)(T * KS_F + fs * 4 + f) * 64 + lane) * 16) = o;
  }
}

constexpr int K5_LDS = 4 * 32768;
template <bool OUT_RAW>
__global__ __launch_bounds__(256) void k_ffn2(const u16* __restrict__ w2, char* __restrict__ ws, char* __restrict__ outp) {
  extern __shared__ __attribute__((aligned(16))) char smem[];
  LAS char* sm = (LAS char*)smem;
  const int b = blockIdx.x, xcd = b & 7, idx = b >> 3;
  const int tp = xcd * 4 + (idx & 3), ns = idx >> 2;
  const int tid = threadIdx.x, lane = tid & 63, w = __builtin_amdgcn_readfirstlane(tid >> 6);
  const int tt = w & 1, kh = w >> 1, T = tp * 2 + tt;
  const unsigned ldsbase = (unsigned)(size_t)smem;
  const int dt = ns * 2 + kh;
  u32x4 rf[2]; u32x2 rr[4];
#pragma unroll
  for (int s = 0; s < 2; ++s) rf[s] = *(const u32x4*)(ws + WS_X1 + ((size_t)(T * KS_D + dt * 2 + s) * 64 + lane) * 16);
  __amdgpu_buffer_rsrc_t rw = mkrsrc(w2, F * D * 2);
  __amdgpu_buffer_rsrc_t rh = mkrsrc(ws + WS_H, NTT * KS_F * 1024);
  const unsigned vw = dma_voff_128(lane, 896, 0);
  const unsigned vh = lane * 16;
  u32x4 hf[4][8];
  auto issue = [&](int c) {
#pragma unroll
    for (int i = 0; i < 8; ++i) {
      const int rloc = 64 * tt + 8 * i;
      dma1k(rw, vw, (kh * 896 + c * 128 + rloc) * 896 + ns * 128, ldsbase + (c & 3) * 32768 + kh * 16384 + rloc * 128);
    }
#pragma unroll
    for (int i = 0; i < 8; ++i) {
      unsigned so = (T * KS_F + kh * 56 + c * 8 + i) * 1024;
      asm volatile("buffer_load_dwordx4 %0, %1, %2, %3 offen" : "=v"(hf[c & 3][i]) : "v"(vh), "s"(rh), "s"(so) : "memory");
    }
  };
  issue(0); issue(1); issue(2);
  const int lane5 = lane >> 5, blk = (lane >> 4) & 1, q = (lane & 15) >> 2, p = lane & 3;
  LAS char* wb[2];
#pragma unroll
  for (int d2 = 0; d2 < 2; ++d2)
    wb[d2] = sm + kh * 16384 + (4 * lane5 + q) * 128 + (((2 * d2 + blk) ^ ((q >> 1) << 1)) << 5) + p * 8;
  f32x16 acc[2] = {};
#pragma unroll
  for (int c = 0; c < 7; ++c) {
    if (c <= 4) asm volatile("s_waitcnt vmcnt(32)" ::: "memory");
    else if (c == 5) asm volatile("s_waitcnt vmcnt(16)" ::: "memory");
    else asm volatile("s_waitcnt vmcnt(0)" ::: "memory");
#pragma unroll
    for (int i = 0; i < 8; ++i) asm volatile("" : "+v"(hf[c & 3][i]));
    BARRIER();
    if (c + 3 < 7) issue(c + 3);
#pragma unroll
    for (int i = 0; i < 8; ++i) {
      const int o = (c & 3) * 32768 + i * 2048;
#pragma unroll
      for (int d2 = 0; d2 < 2; ++d2) acc[d2] = MFMA(ld_tr(wb[d2], o, o + 1024), as_bf(hf[c & 3][i]), acc[d2]);
    }
  }
  BARRIER();
  LAS f32x4* ex = (LAS f32x4*)(sm) + (size_t)(tt * 2 + kh) * 256;
  {
    const f32x16& give = kh ? acc[0] : acc[1];
#pragma unroll
    for (int g = 0; g < 4; ++g) ex[g * 64 + lane] = f32x4{give[4 * g], give[4 * g + 1], give[4 * g + 2], give[4 * g + 3]};
  }
  BARRIER();
  f32x16 keep = kh ? acc[1] : acc[0];
  {
    LAS f32x4* rd = (LAS f32x4*)(sm) + (size_t)(tt * 2 + (kh ^ 1)) * 256;
#pragma unroll
    for (int g = 0; g < 4; ++g) { f32x4 v = rd[g * 64 + lane]; keep[4 * g] += v[0]; keep[4 * g + 1] += v[1]; keep[4 * g + 2] += v[2]; keep[4 * g + 3] += v[3]; }
  }
  res_epilogue<false, OUT_RAW>(keep, rf, rr, T, dt, lane, outp, (f32x2*)(ws + WS_ST2));
}

extern "C" void kernel_launch(void* const* d_in, const int* in_sizes, int n_in, void* d_out, int out_size, void* d_ws,
                              size_t ws_size, hipStream_t stream) {
  const char* x = (const char*)d_in[0];
  const u16* wqkv = (const u16*)d_in[1];
  const u16* wo = (const u16*)d_in[2];
  const u16* w1 = (const u16*)d_in[4];
  const u16* w3 = (const u16*)d_in[5];
  const u16* w2 = (const u16*)d_in[6];
  char* ws = (char*)d_ws;
  (void)hipFuncSetAttribute((const void*)k_qkv<true>, hipFuncAttributeMaxDynamicSharedMemorySize, K1_LDS);
  (void)hipFuncSetAttribute((const void*)k_qkv<false>, hipFuncAttributeMaxDynamicSharedMemorySize, K1_LDS);
  (void)hipFuncSetAttribute((const void*)k_ffn1, hipFuncAttributeMaxDynamicSharedMemorySize, K4_LDS);
  (void)hipFuncSetAttribute((const void*)k_ffn2<true>, hipFuncAttributeMaxDynamicSharedMemorySize, K5_LDS);
  (void)hipFuncSetAttribute((const void*)k_ffn2<false>, hipFuncAttributeMaxDynamicSharedMemorySize, K5_LDS);
  k_qkv<true><<<256, 256, K1_LDS, stream>>>(x, nullptr, wqkv, ws);
  k_attn<<<256, 256, 0, stream>>>(ws);
  k_wo<true><<<224, 256, K3_LDS, stream>>>(x, wo, ws);
  k_ffn1<<<224, 512, K4_LDS, stream>>>(w1, w3, ws);
  k_ffn2<false><<<224, 256, K5_LDS, stream>>>(w2, ws, ws + WS_X2);
  k_qkv<false><<<256, 256, K1_LDS, stream>>>(ws + WS_X2, (const f32x2*)(ws + WS_ST2), wqkv, ws);
  k_attn<<<256, 256, 0, stream>>>(ws);
  k_wo<false><<<224, 256, K3_LDS, stream>>>(ws + WS_X2, wo, ws);
  k_ffn1<<<224, 512, K4_LDS, stream>>>(w1, w3, ws);
  k_ffn2<true><<<224, 256, K5_LDS, stream>>>(w2, ws, (char*)d_out);
}
```

```cpp
#include <hip/hip_runtime.h>

typedef unsigned long long u64;
typedef __attribute__((ext_vector_type(8))) __bf16 bf16x8;
typedef __attribute__((ext_vector_type(2))) __bf16 bf16x2;
typedef __attribute__((ext_vector_type(4))) short s16x4;
typedef __attribute__((ext_vector_type(16))) float f32x16;
typedef __attribute__((ext_vector_type(4))) float f32x4;
typedef __attribute__((ext_vector_type(4))) unsigned u32x4;
typedef __attribute__((ext_vector_type(2))) unsigned u32x2;
#define LAS __attribute__((address_space(3)))
#define GAS __attribute__((address_space(1)))
#define DI __device__ __forceinline__
#define MFMA(a, b, c) __builtin_amdgcn_mfma_f32_32x32x16_bf16((a), (b), (c), 0, 0, 0)
#define RLX_AGENT __ATOMIC_RELAXED, __HIP_MEMORY_SCOPE_AGENT
#define CBAR() asm volatile("" ::: "memory")
#define OPQ_V(x) asm volatile("" : "+v"(x))
#define PIN(x) asm volatile("" : "+v"(x))
#define OPQ_S(x) asm volatile("" : "+s"(x))
#define RFL(x) __builtin_amdgcn_readfirstlane(x)
#define SCHED0() __builtin_amdgcn_sched_barrier(0)

constexpr int S = 2048, D = 448, NH = 16, F = 1792;
constexpr int NTT = S / 32;
constexpr int KS_D = D / 16;
constexpr int KS_F = F / 16;
constexpr int KS_O = NH * 2;
constexpr float LN_EPS = 1e-5f;
constexpr float LOG2E = 1.4426950408889634f;
constexpr float QSCALE = 0.18898223650461362f * LOG2E;

constexpr int DEPTH = 11;
constexpr int PF2 = 2;
constexpr int XAHEAD = 12, HAHEAD = 12;
constexpr unsigned SZ_X = NTT * KS_D * 1024, SZ_KV = NH * NTT * 2048, SZ_O = NTT * KS_O * 1024, SZ_H = NTT * KS_F * 1024;
constexpr unsigned SZ_ST = NTT * 14 * 32 * 8;
constexpr unsigned WS_X = 0;
constexpr unsigned WS_ST = WS_X + 4 * SZ_X;
constexpr unsigned WS_K = WS_ST + 4 * SZ_ST;
constexpr unsigned WS_V = WS_K + 2 * SZ_KV;
constexpr unsigned WS_O = WS_V + 2 * SZ_KV;
constexpr unsigned WS_H = WS_O + 2 * SZ_O;
constexpr unsigned WS_DATA_END = WS_H + 2 * SZ_H;
constexpr unsigned WS_WT1 = (WS_DATA_END + 4095u) & ~4095u;
constexpr unsigned WS_WT2 = WS_WT1 + 16 * 87 * 1024;
constexpr unsigned WS_WT3 = WS_WT2 + 7 * 64 * 1024;
constexpr unsigned WS_WT4 = WS_WT3 + 16 * 203 * 1024;
constexpr unsigned WS_FLAGS = WS_WT4 + 7 * 224 * 1024;
constexpr unsigned FL_KV = 0;
constexpr unsigned FL_O = FL_KV + 2 * 1024;
constexpr unsigned FL_X = FL_O + 2 * 1024;
constexpr unsigned FL_H = FL_X + 4 * 1024;
constexpr unsigned FL_P = FL_H + 6 * 1024;
constexpr unsigned FL_NUM = FL_P + 288;
constexpr unsigned WS_ARRIVE = WS_FLAGS + FL_NUM * 128;
constexpr unsigned WS_EPOCH = WS_ARRIVE + 2048 * 8;
constexpr unsigned WS_END = WS_EPOCH + 128;

constexpr int NCH = 38;
constexpr int CH_KV = 0, CH_Q = 4, CH_WO = 6, CH_A = 10, CH_B = 16, CH_E = 20, CH_C = 24;
__device__ constexpr int CH_ADDR0[NCH] = {0, 16, 32, 48, 58, 74, 87, 103, 119, 0, 16, 31, 46, 61, 76, 91, 103, 119, 0, 16, 26, 42, 58, 74, 84, 100, 116, 0, 16, 32, 48, 64, 80, 96, 112, 0, 16, 32};
__device__ constexpr int CH_ADDR1[NCH] = {48, 64, 80, 96, 106, 122, 0, 16, 32, 48, 64, 79, 94, 109, 124, 0, 12, 28, 44, 60, 70, 86, 102, 118, 0, 16, 32, 48, 64, 80, 96, 112, 0, 16, 32, 48, 64, 80};
__device__ constexpr unsigned CH_TAB[2 * NCH] = {0x00100000u, 0x00101000u, 0x00102000u, 0x000a3000u, 0x00103a00u, 0x000d4a00u, 0x01105700u, 0x01106700u, 0x01107700u, 0x01100001u, 0x020f1002u, 0x020f1f03u, 0x020f2e05u, 0x020f3d06u, 0x020f4c07u, 0x020c5b07u, 0x02106708u, 0x02107709u, 0x0210000au, 0x020a100bu, 0x02101a0cu, 0x02102a0du, 0x02103a0eu, 0x020a4a0fu, 0x03105410u, 0x03106411u, 0x03107412u, 0x03100013u, 0x03101015u, 0x03102016u, 0x03103017u, 0x03104018u, 0x03105019u, 0x0310601au, 0x0310701bu, 0x0310001cu, 0x0310101du, 0x0310201eu, 0x0010301fu, 0x00104020u, 0x00105021u, 0x000a6022u, 0x00106a23u, 0x000d7a23u, 0x01100024u, 0x01101025u, 0x01102026u, 0x01103027u, 0x020f4028u, 0x020f4f29u, 0x020f5e2bu, 0x020f6d2cu, 0x020f7c2cu, 0x020c002du, 0x02100c2eu, 0x02101c2fu, 0x02102c30u, 0x020a3c31u, 0x02104632u, 0x02105633u, 0x02106634u, 0x020a7635u, 0x03100037u, 0x03101038u, 0x03102039u, 0x0310303au, 0x0310403bu, 0x0310503cu, 0x0310603du, 0x0310703eu, 0x0310003fu, 0x03101040u, 0x03102041u, 0x03103042u, 0x03104043u, 0x03105044u};
__device__ constexpr unsigned CH_SRC[NCH] = {0, 16, 32, 48, 58, 74, 0, 16, 32, 48, 0, 15, 30, 45, 60, 75, 87, 103, 119, 135, 145, 161, 177, 193, 0, 16, 32, 48, 64, 80, 96, 112, 128, 144, 160, 176, 192, 208};
constexpr int LDS_RING = 142 * 1024;
constexpr int LDS_EXCH = LDS_RING;
constexpr int LDS_MISC = LDS_EXCH + 16384;
constexpr int LDS_FILLED = LDS_MISC, LDS_CONSUMED = LDS_MISC + 16, LDS_EXF = LDS_MISC + 32, LDS_MODE = LDS_MISC + 48;
constexpr int LDS_RSTD = LDS_MISC + 64;
constexpr int LDS_TOTAL = LDS_RSTD + 512;
static_assert(LDS_TOTAL <= 163840, "LDS");
constexpr unsigned SPIN_CAP = 1u << 16;

template <bool B> struct BoolC { static constexpr bool value = B; };
struct Params {
  const char* x; const char* wqkv; const char* wo; const char* w1; const char* w3; const char* w2; char* ws; char* out;
};


DI bf16x8 as_bf(u32x4 v) { return __builtin_bit_cast(bf16x8, v); }
DI unsigned pk2(float a, float b) { bf16x2 p = {(__bf16)a, (__bf16)b}; return __builtin_bit_cast(unsigned, p); }
DI float bf_lo(unsigned v) { return __uint_as_float(v << 16); }
DI float bf_hi(unsigned v) { return __uint_as_float(v & 0xffff0000u); }
DI float rbf(float a) { return (float)(__bf16)a; }
DI float xhalf_sum(float x) {
  auto r = __builtin_amdgcn_permlane32_swap(__float_as_uint(x), __float_as_uint(x), false, false);
  return __uint_as_float(r[0]) + __uint_as_float(r[1]);
}
DI float xhalf_max(float x) {
  auto r = __builtin_amdgcn_permlane32_swap(__float_as_uint(x), __float_as_uint(x), false, false);
  return fmaxf(__uint_as_float(r[0]), __uint_as_float(r[1]));
}
DI __amdgpu_buffer_rsrc_t mkrsrc(const void* p, unsigned bytes) {
  return __builtin_amdgcn_make_buffer_rsrc((void*)p, (short)0, (int)bytes, 0x00020000);
}
DI u32x4 ld16_sc1(__amdgpu_buffer_rsrc_t r, unsigned voff, unsigned soff) { return __builtin_amdgcn_raw_buffer_load_b128(r, voff, soff, 16); }
DI u32x2 ld8_sc1(__amdgpu_buffer_rsrc_t r, unsigned voff, unsigned soff) { return __builtin_amdgcn_raw_buffer_load_b64(r, voff, soff, 16); }
DI void st16_sc1(__amdgpu_buffer_rsrc_t r, unsigned voff, unsigned soff, u32x4 v) { __builtin_amdgcn_raw_buffer_store_b128(v, r, voff + soff, 0, 16); }
DI void st8_sc1(__amdgpu_buffer_rsrc_t r, unsigned voff, unsigned soff, u32x2 v) { __builtin_amdgcn_raw_buffer_store_b64(v, r, voff, soff, 16); }
DI int wstart(int head) { return 56 * (head >> 1) + 24 * (head & 1); }
DI u32x4 pack8(const f32x16& a, int s) {
  u32x4 r;
  r[0] = pk2(a[8 * s + 0], a[8 * s + 1]); r[1] = pk2(a[8 * s + 2], a[8 * s + 3]);
  r[2] = pk2(a[8 * s + 4], a[8 * s + 5]); r[3] = pk2(a[8 * s + 6], a[8 * s + 7]);
  return r;
}
DI u32x4 corr_frag_mean(float mean, int h) {
  float hi = rbf(-mean), lo = rbf(-mean - hi);
  unsigned p = pk2(hi, lo);
  u32x4 r; r[0] = h ? 0u : p; r[1] = h ? 0u : p; r[2] = 0u; r[3] = 0u;
  return r;
}
DI u32x4 corr_frag_cs(float cs, int h) {
  float hi = rbf(cs), lo = rbf(cs - hi);
  u32x4 r; r[0] = h ? 0u : pk2(hi, hi); r[1] = h ? 0u : pk2(lo, lo); r[2] = 0u; r[3] = 0u;
  return r;
}
DI void lds_store32(unsigned addr, unsigned v) { asm volatile("ds_write_b32 %0, %1" ::"v"(addr), "v"(v) : "memory"); }
DI u32x4 lds_load128(unsigned addr) {
  u32x4 r;
  asm volatile("ds_read_b128 %0, %1\n\ts_waitcnt lgkmcnt(0)" : "=v"(r) : "v"(addr) : "memory");
  return r;
}
DI unsigned lds_load32(unsigned addr) {
  unsigned r;
  asm volatile("ds_read_b32 %0, %1\n\ts_waitcnt lgkmcnt(0)" : "=v"(r) : "v"(addr) : "memory");
  return r;
}
DI unsigned min4u(u32x4 f) { unsigned a = f[0] < f[1] ? f[0] : f[1], b = f[2] < f[3] ? f[2] : f[3]; return RFL(a < b ? a : b); }

#define W3POS(f) ((f) < 3 ? (f) : (f) < 5 ? 87 + (f) - 3 : 145 + (f) - 5)
#define W3STEP(f) ((f) < 3 ? 3 : 2)
DI u32x4 ld_tr(LAS char* p, int off0, int off1) {
  s16x4 a = __builtin_amdgcn_ds_read_tr16_b64_v4i16((LAS s16x4*)(p + off0));
  s16x4 b = __builtin_amdgcn_ds_read_tr16_b64_v4i16((LAS s16x4*)(p + off1));
  return __builtin_bit_cast(u32x4, __builtin_shufflevector(a, b, 0, 1, 2, 3, 4, 5, 6, 7));
}
DI float dot_ones(u32x4 u, float c) {
#pragma unroll
  for (int j = 0; j < 4; ++j) { const unsigned e = u[j]; c = __builtin_amdgcn_fdot2_f32_bf16(__builtin_bit_cast(bf16x2, e), __builtin_bit_cast(bf16x2, 0x3f803f80u), c, false); }
  return c;
}
DI void setup_part1(const Params& p, __amdgpu_buffer_rsrc_t rw, int b, int w, int lane, LAS char* stg, LAS float* red) {
  const int t = lane & 31, h = lane >> 5;
  const int lr = lane >> 2, lc = lane & 3;
  LAS char* trp = stg + (4 * h + ((lane & 15) >> 2)) * 64 + ((lane >> 4) & 1) * 32 + (lane & 3) * 8;
  if (b < 160) {
    const char* base; int pitch; bool valid = true; unsigned dst; int dstep;
    if (b < 48) {
      const int head = b / 3, sec = b % 3;
      valid = (head & 1) ? t >= 4 : t < 28;
      base = p.wqkv + ((sec == 2 ? 0 : (sec + 1) * D) + wstart(head)) * 2 + lc * 16; pitch = 3 * D * 2;
      dst = WS_WT1 + head * 87 * 1024 + (sec == 2 ? 58 : sec) * 1024; dstep = sec == 2 ? 1024 : 2048;
    } else {
      const int id = b - 48, j = id / 7, f = id % 7;
      base = ((lc & 2) ? p.w3 : p.w1) + (112 * j + 16 * f) * 2 + (lc & 1) * 16; pitch = F * 2;
      dst = WS_WT3 + j * 203 * 1024 + W3POS(f) * 1024; dstep = W3STEP(f) * 1024;
    }
    u32x4 v[7];
#pragma unroll
    for (int i = 0; i < 7; ++i) v[i] = *(const u32x4*)(base + (size_t)(16 * (7 * w + i) + lr) * pitch);
#pragma unroll
    for (int i = 0; i < 7; ++i) *(LAS u32x4*)(stg + i * 1024 + lane * 16) = v[i];
    float sum = 0.f;
#pragma unroll
    for (int i = 0; i < 7; ++i) {
      u32x4 fr = ld_tr(trp, i * 1024, i * 1024 + 512);
      if (!valid) fr = u32x4{0u, 0u, 0u, 0u};
      sum = dot_ones(fr, sum);
      st16_sc1(rw, lane * 16, dst + (7 * w + i) * dstep, fr);
    }
    sum = xhalf_sum(sum);
    if (h == 0) red[(w * 32 + t) * 2] = sum;
  } else if (b < 223) {
    u32x4 v[8];
    unsigned dsts[8];
    int zmode[8];
#pragma unroll
    for (int i = 0; i < 8; ++i) {
      const int n = ((b - 160) * 4 + w) * 8 + i;
      const char* src; int row0;
      zmode[i] = 0;
      if (n < 448) {
        const int ns = n / 64, r = n % 64, c = r / 16, kh = (r % 16) / 8, ks4 = (r % 8) / 2, d2 = r & 1;
        const int st = 4 * c + ks4, head = 8 * kh + (st >> 1), s = st & 1;
        src = p.wo + (ns * 64 + d2 * 32) * 2; row0 = wstart(head) + 16 * s;
        zmode[i] = (head & 1) ? (s == 0 ? 2 : 0) : (s == 1 ? 1 : 0);
        dsts[i] = WS_WT2 + n * 1024;
      } else {
        const int m = n - 448, ns = m / 224, r = m % 224;
        const int kh = (r % 16) / 8, d2 = r & 1, st = 4 * (r / 16) + (r % 8) / 2;
        const int fb = st < 24 ? (8 * kh + st / 3) * 7 + st % 3 : st < 40 ? (8 * kh + (st - 24) / 2) * 7 + 3 + (st & 1) : (8 * kh + (st - 40) / 2) * 7 + 5 + (st & 1);
        src = p.w2 + (ns * 64 + d2 * 32) * 2; row0 = 16 * fb;
        dsts[i] = WS_WT4 + m * 1024;
      }
      v[i] = *(const u32x4*)(src + (size_t)(row0 + lr) * (D * 2) + lc * 16);
    }
#pragma unroll
    for (int i = 0; i < 8; ++i) *(LAS u32x4*)(stg + i * 1024 + lane * 16) = v[i];
#pragma unroll
    for (int i = 0; i < 8; ++i) {
      u32x4 fr = ld_tr(trp, i * 1024, i * 1024 + 512);
      if (zmode[i] == 1 && h == 1) { fr[2] = 0u; fr[3] = 0u; }
      if (zmode[i] == 2 && h == 0) { fr[0] = 0u; fr[1] = 0u; }
      st16_sc1(rw, lane * 16, dsts[i], fr);
    }
  } else if (b < 287) {
    const int T = b - 223;
    u32x4 v[7];
    int so[7];
    int row = lane / 14, cc = lane - row * 14;
#pragma unroll
    for (int i = 0; i < 7; ++i) {
      v[i] = *(const u32x4*)(p.x + ((size_t)(T * 32 + row) * D + 112 * w) * 2 + cc * 16);
      so[i] = row * 240 + cc * 16;
      cc += 8; row += 4;
      if (cc >= 14) { cc -= 14; row += 1; }
    }
#pragma unroll
    for (int i = 0; i < 7; ++i) *(LAS u32x4*)(stg + so[i]) = v[i];
    float sum = 0.f, sq = 0.f;
#pragma unroll
    for (int i = 0; i < 7; ++i) {
      LAS char* q = stg + t * 240 + i * 32 + 8 * h;
      const u32x2 a = *(LAS u32x2*)q, c = *(LAS u32x2*)(q + 16);
      const u32x4 fr = {a[0], a[1], c[0], c[1]};
      st16_sc1(rw, lane * 16, WS_X + (T * KS_D + 7 * w + i) * 1024, fr);
#pragma unroll
      for (int j = 0; j < 4; ++j) { const float lo = bf_lo(fr[j]), hi = bf_hi(fr[j]); sum += lo + hi; sq = fmaf(lo, lo, sq); sq = fmaf(hi, hi, sq); }
    }
    sum = xhalf_sum(sum); sq = xhalf_sum(sq);
    if (h == 0) { red[(w * 32 + t) * 2] = sum; red[(w * 32 + t) * 2 + 1] = sq; }
  }
}
DI void setup_part2(__amdgpu_buffer_rsrc_t rw, int b, int w, int lane, LAS float* red) {
  const int t = lane & 31, h = lane >> 5;
  const float s0 = (red[t * 2] + red[(32 + t) * 2]) + (red[(64 + t) * 2] + red[(96 + t) * 2]);
  if (b < 160) {
    if (w == 0) {
      unsigned dst;
      if (b < 48) { const int head = b / 3, sec = b % 3; dst = WS_WT1 + head * 87 * 1024 + (sec == 2 ? 58 + 28 : 56 + sec) * 1024; }
      else { const int id = b - 48, j = id / 7, f = id % 7; dst = WS_WT3 + j * 203 * 1024 + (W3POS(f) + 28 * W3STEP(f)) * 1024; }
      st16_sc1(rw, lane * 16, dst, corr_frag_cs(s0, h));
    }
  } else if (b >= 223 && b < 287) {
    const int T = b - 223;
    const float s1 = (red[t * 2 + 1] + red[(32 + t) * 2 + 1]) + (red[(64 + t) * 2 + 1] + red[(96 + t) * 2 + 1]);
#pragma unroll
    for (int i = 0; i < 4; ++i) {
      const int pi = w + 4 * i;
      if (pi < 14) st8_sc1(rw, t * 8 + (h ? 0x80000000u : 0u), WS_ST + (T * 14 + pi) * 256, pi == 0 ? u32x2{__float_as_uint(s0), __float_as_uint(s1)} : u32x2{0u, 0u});
    }
  }
}

DI void wait_flags(char* ws, unsigned fi, int n, u64 tag, int lane) {
  GAS u64* f = (GAS u64*)(ws + WS_FLAGS) + (size_t)(fi + (lane < n ? lane : 0)) * 16;
#pragma unroll 1
  for (unsigned spins = 0; spins < SPIN_CAP; ++spins) {
    u64 v = __hip_atomic_load(f, RLX_AGENT);
    if (__all(v == tag)) break;
  }
  CBAR();
}
struct Flags {
  GAS u64* f; u64 tag; unsigned mask;
  DI void init(char* ws, unsigned fi, int n, u64 tg, int lane) { f = (GAS u64*)(ws + WS_FLAGS) + (size_t)(fi + (lane < n ? lane : 0)) * 16; tag = tg; mask = 0; }
  DI void ready_all(int n) {
    const unsigned full = (1u << n) - 1u;
    __builtin_amdgcn_s_sleep(20);
#pragma unroll 1
    for (unsigned spins = 0; spins < SPIN_CAP && (mask & full) != full; ++spins) {
      const u64 v = __hip_atomic_load(f, RLX_AGENT);
      mask = (unsigned)__ballot(v == tag);
    }
    CBAR();
  }
  DI void ready(int k) {
    if (k == 0) __builtin_amdgcn_s_sleep(16);
#pragma unroll 1
    for (unsigned spins = 0; spins < SPIN_CAP && !((mask >> k) & 1u); ++spins) {
      const u64 v = __hip_atomic_load(f, RLX_AGENT);
      mask = (unsigned)__ballot(v == tag);
    }
    CBAR();
  }
};
DI void set_flag(char* ws, unsigned fi, u64 tag, int lane) {
  if (lane == 0) __hip_atomic_store((GAS u64*)(ws + WS_FLAGS) + (size_t)fi * 16, tag, RLX_AGENT);
}
DI void publish(char* ws, unsigned fi, u64 tag, int lane) {
  asm volatile("s_waitcnt vmcnt(0)" ::: "memory");
  set_flag(ws, fi, tag, lane);
}

template <int V> struct IntC { static constexpr int value = V; };
template <int I, int N, class FN> DI void sfor(FN&& fn) { if constexpr (I < N) { fn(IntC<I>{}); sfor<I + 1, N>(fn); } }
template <int N, int NF, int PF, class LD, class MM, class POST, class MID>
DI void pipe(LD ld, MM mm, POST post, MID mid) {
  bf16x8 buf[PF + 1][NF];
#pragma unroll
  for (int i = 0; i < PF; ++i)
#pragma unroll
    for (int f = 0; f < NF; ++f) buf[i][f] = ld(i, f);
  SCHED0();
  mid();
  sfor<0, N>([&](auto ic) {
    constexpr int i = decltype(ic)::value;
#pragma unroll
    for (int f = 0; f < NF; ++f) {
      SCHED0();
      mm(i, f, buf[i % (PF + 1)][f]);
      SCHED0();
      if (i + PF < N) buf[(i + PF) % (PF + 1)][f] = ld(i + PF, f);
    }
    post(i);
  });
  SCHED0();
}
template <int N, int NF, int PF, class LD, class MM, class POST>
DI void pipe(LD ld, MM mm, POST post) { pipe<N, NF, PF>(ld, mm, post, []() {}); }

struct LD {
  __amdgpu_buffer_rsrc_t rs;
  unsigned base[4];
  unsigned ldsbase, seen; int lw, lane, j, ns; char* ws; u64 tag;
  u32x4 r[DEPTH][4];
};
template <int G> DI void ld_issue(LD& L) {
  if constexpr (G < 2 * NCH) {
    constexpr unsigned e = CH_TAB[G], nf = (e >> 16) & 255u, kind = e >> 24, so = CH_SRC[G % NCH];
    if constexpr (G == CH_KV) wait_flags(L.ws, FL_P + 3 * L.j, 3, L.tag, L.lane);
    if constexpr (G == CH_WO) wait_flags(L.ws, FL_P + 160 + 2 * L.ns, 2, L.tag, L.lane);
    if constexpr (G == CH_A) wait_flags(L.ws, FL_P + 48 + 7 * L.j, 7, L.tag, L.lane);
    if constexpr (G == CH_C) wait_flags(L.ws, FL_P + 174 + 7 * L.ns, 7, L.tag, L.lane);
    unsigned b = L.base[kind]; OPQ_S(b);
#pragma unroll
    for (int k = 0; k < 4; ++k) {
      int f = L.lw + 4 * k;
      if (4 * k + 3 >= (int)nf) f = f < (int)nf ? f : (int)nf - 1;
      L.r[G % DEPTH][k] = __builtin_amdgcn_raw_buffer_load_b128(L.rs, L.lane * 16, b + (so + f) * 1024u, 16);
    }
  }
}
template <int G> DI void ld_step(LD& L) {
  constexpr unsigned e = CH_TAB[G], dep = e & 255u, addr = (e >> 8) & 255u, nf = (e >> 16) & 255u;
  if (L.seen < dep) {
#pragma unroll 1
    for (unsigned spins = 0; spins < SPIN_CAP * 8; ++spins) {
      L.seen = min4u(lds_load128(L.ldsbase + LDS_CONSUMED));
      if (L.seen >= dep) break;
      __builtin_amdgcn_s_sleep(2);
    }
  }
  int ln = L.lane; OPQ_V(ln);
  LAS char* dst = (LAS char*)(size_t)(L.ldsbase + addr * 1024u) + ln * 16;
#pragma unroll
  for (int k = 0; k < 4; ++k) {
    if (4 * k + 3 < (int)nf) *(LAS u32x4*)(dst + L.lw * 1024 + k * 4096) = L.r[G % DEPTH][k];
    else { int f = L.lw + 4 * k; f = f < (int)nf ? f : (int)nf - 1; *(LAS u32x4*)(dst + f * 1024) = L.r[G % DEPTH][k]; }
  }
  lds_store32(L.ldsbase + LDS_FILLED + L.lw * 4, G + 1);
  ld_issue<G + DEPTH>(L);
  if constexpr (G + 1 < 2 * NCH) ld_step<G + 1>(L);
}
template <int G, int E> DI void ld_prologue(LD& L) { if constexpr (G < E) { ld_issue<G>(L); ld_prologue<G + 1, E>(L); } }
DI void loader_main(const Params& p, int lw, int lane, int j, unsigned ldsbase, u64 tag) {
  LD L;
  const int ns = j < 14 ? j >> 1 : 0;
  L.rs = mkrsrc(p.ws, WS_FLAGS);
  L.base[0] = WS_WT1 + j * 87 * 1024; L.base[1] = WS_WT2 + ns * 64 * 1024;
  L.base[2] = WS_WT3 + j * 203 * 1024; L.base[3] = WS_WT4 + ns * 224 * 1024;
  L.ldsbase = ldsbase; L.seen = 0; L.lw = lw; L.lane = lane; L.j = j; L.ns = ns; L.ws = p.ws; L.tag = tag;
  ld_prologue<0, 4>(L);
#pragma unroll 1
  for (unsigned spins = 0; spins < SPIN_CAP; ++spins) {
    const u32x4 m = lds_load128(ldsbase + LDS_MODE);
    if (RFL(m[0] & m[1] & m[2] & m[3]) != 0u) break;
    __builtin_amdgcn_s_sleep(1);
  }
  ld_prologue<4, DEPTH>(L);
  ld_step<0>(L);
  if (blockIdx.x == 255 && lw == 3) {
#pragma unroll 1
    for (unsigned spins = 0; spins < SPIN_CAP; ++spins) {
      unsigned bad = 0;
#pragma unroll
      for (int i = 0; i < 32; ++i) bad |= __hip_atomic_load((GAS u64*)(p.ws + WS_ARRIVE) + i * 64 + lane, RLX_AGENT) != tag;
      if (__all(bad == 0u)) break;
    }
    if (lane == 0) __hip_atomic_store((GAS u64*)(p.ws + WS_EPOCH), tag, RLX_AGENT);
  }
}

struct CW {
  LAS char* sm; unsigned ldsbase; int lane, w, j, grp; unsigned have; unsigned exn; u64 tag; u32x4 pk;
};
#define PHASE_IDS() OPQ_V(c.lane); c.w = RFL(c.w); c.j = RFL(c.j); c.grp = RFL(c.grp); OPQ_S(c.w); OPQ_S(c.j); OPQ_S(c.grp); \
  const int lane = c.lane, w = c.w, cj = c.j, grp = c.grp; const int t = lane & 31, h = lane >> 5; (void)t; (void)h; (void)cj; (void)grp

DI void peek(CW& c) {
  const u32x4 f = c.pk;
  const unsigned a = f[0] < f[1] ? f[0] : f[1], b = f[2] < f[3] ? f[2] : f[3], m = RFL(a < b ? a : b);
  c.have = m > c.have ? m : c.have;
  c.pk = *(volatile LAS u32x4*)(c.sm + LDS_FILLED);
}
DI void need(CW& c, unsigned g) {
  peek(c);
  if (c.have > g) return;
#pragma unroll 1
  for (unsigned spins = 0; spins < SPIN_CAP * 8; ++spins) {
    c.have = min4u(lds_load128(c.ldsbase + LDS_FILLED));
    if (c.have > g) break;
    __builtin_amdgcn_s_sleep(1);
  }
}
DI void release(CW& c, unsigned g) { lds_store32(c.ldsbase + LDS_CONSUMED + c.w * 4, g + 1); }
DI LAS char* chunk(CW& c, int layer, int ch) { return c.sm + c.lane * 16 + (layer ? CH_ADDR1[ch] : CH_ADDR0[ch]) * 1024; }
DI bf16x8 ldw(LAS char* p, int frag) { return *(LAS bf16x8*)(p + frag * 1024); }

DI f32x16 pair_exchange(CW& c, const f32x16 (&acc)[2], int kh) {
  LAS f32x4* ex = (LAS f32x4*)(c.sm + LDS_EXCH + c.w * 4096);
  const f32x16 give = kh ? acc[0] : acc[1];
#pragma unroll
  for (int g = 0; g < 4; ++g) ex[g * 64 + c.lane] = f32x4{give[4 * g], give[4 * g + 1], give[4 * g + 2], give[4 * g + 3]};
  c.exn += 1;
  lds_store32(c.ldsbase + LDS_EXF + c.w * 4, c.exn);
  const int pw = c.w ^ 2;
#pragma unroll 1
  for (unsigned spins = 0; spins < SPIN_CAP * 8; ++spins)
    if (RFL(lds_load32(c.ldsbase + LDS_EXF + pw * 4)) >= c.exn) break;
  f32x16 keep = kh ? acc[1] : acc[0];
  LAS f32x4* rd = (LAS f32x4*)(c.sm + LDS_EXCH + pw * 4096);
#pragma unroll
  for (int g = 0; g < 4; ++g) { f32x4 v = rd[g * 64 + c.lane]; keep[4 * g] += v[0]; keep[4 * g + 1] += v[1]; keep[4 * g + 2] += v[2]; keep[4 * g + 3] += v[3]; }
  return keep;
}
DI void add_res(f32x16& a, const u32x4 (&rf)[2]) {
#pragma unroll
  for (int s = 0; s < 2; ++s)
#pragma unroll
    for (int j = 0; j < 4; ++j) { a[8 * s + 2 * j] += bf_lo(rf[s][j]); a[8 * s + 2 * j + 1] += bf_hi(rf[s][j]); }
}
DI void store_x_tile(const u32x4 (&o)[2], __amdgpu_buffer_rsrc_t rs, int xb, int T, int dt, int lane) {
  const unsigned xo = WS_X + xb * SZ_X + (T * KS_D + dt * 2) * 1024;
  st16_sc1(rs, lane * 16, xo, o[0]);
  st16_sc1(rs, lane * 16 + 1024, xo, o[1]);
  float sum = 0.f, sq = 0.f;
#pragma unroll
  for (int j = 0; j < 4; ++j) {
    float a = bf_lo(o[0][j]), b = bf_hi(o[0][j]), e = bf_lo(o[1][j]), f = bf_hi(o[1][j]);
    sum += (a + b) + (e + f);
    sq = fmaf(a, a, sq); sq = fmaf(b, b, sq); sq = fmaf(e, e, sq); sq = fmaf(f, f, sq);
  }
  sum = xhalf_sum(sum); sq = xhalf_sum(sq);
  st8_sc1(rs, (lane & 31) * 8 + ((lane >> 5) ? 0x80000000u : 0u), WS_ST + xb * SZ_ST + (T * 14 + dt) * 256, u32x2{__float_as_uint(sum), __float_as_uint(sq)});
}
DI u32x4 ld_xfrag(__amdgpu_buffer_rsrc_t rs, int xb, int T, int lane, int i) {
  return ld16_sc1(rs, lane * 16 + (i & 3) * 1024, WS_X + xb * SZ_X + (T * KS_D + (i & ~3)) * 1024);
}
DI void x_first(char* ws, u64 tag, __amdgpu_buffer_rsrc_t rs, int xb, int T, int lane, u32x4 (&xf)[KS_D + 1]) {
  Flags fl; fl.init(ws, FL_X + (xb * NTT + T) * 16, 14, tag, lane);
  fl.ready_all(14);
#pragma unroll
  for (int i = 0; i < XAHEAD; ++i) xf[i] = ld_xfrag(rs, xb, T, lane, i);
  SCHED0();
}
DI void x_next(__amdgpu_buffer_rsrc_t rs, int xb, int T, int lane, int i, u32x4 (&xf)[KS_D + 1], u32x2 (&stv)[7]) {
  if (i + XAHEAD < KS_D) xf[i + XAHEAD] = ld_xfrag(rs, xb, T, lane, i + XAHEAD);
  if (i + XAHEAD == KS_D) {
    const int t = lane & 31, h = lane >> 5;
#pragma unroll
    for (int q = 0; q < 7; ++q) stv[q] = ld8_sc1(rs, (h * 7 * 32 + t) * 8, WS_ST + xb * SZ_ST + (T * 14 + q) * 256);
  }
}
DI float finish_stats(const u32x2 (&stv)[7], int h, u32x4& mf) {
  float sum = 0.f, sq = 0.f;
#pragma unroll
  for (int i = 0; i < 7; ++i) { sum += __uint_as_float(stv[i][0]); sq += __uint_as_float(stv[i][1]); }
  sum = xhalf_sum(sum); sq = xhalf_sum(sq);
  const float mean = sum * (1.f / D);
  const float var = fmaxf(sq * (1.f / D) - mean * mean, 0.f);
  mf = corr_frag_mean(rbf(mean), h);
  const float sd = rbf(__builtin_sqrtf(rbf(rbf(var) + LN_EPS)));
  return sd * sd;
}

DI void phase_t1(const Params& p, CW& c, int layer, __amdgpu_buffer_rsrc_t rs) {
  PHASE_IDS();
  const int head = cj, T = grp * 4 + w;
  const unsigned g0 = layer * NCH;
  u32x4 xf[KS_D + 1];
  float var = 0.f;
  u32x2 stv[7];
  if (layer) x_first(p.ws, c.tag, rs, 2, T, lane, xf);
  else {
    wait_flags(p.ws, FL_P + 223 + T, 1, c.tag, lane);
#pragma unroll
    for (int i = 0; i < XAHEAD; ++i) xf[i] = ld_xfrag(rs, 0, T, lane, i);
    SCHED0();
    lds_store32(c.ldsbase + LDS_MODE + w * 4, 1u);
  }
  LAS float* rsl = (LAS float*)(c.sm + LDS_RSTD) + w * 32;
  f32x16 ak = {}, av = {}, aq = {};
  pipe<KS_D + 1, 2, PF2>(
      [&](int i, int f) -> bf16x8 {
        if ((i & 15) == 0 && f == 0) need(c, g0 + CH_KV + (i >> 3) + 1);
        return ldw(chunk(c, layer, CH_KV + (i >> 3)), (i & 7) * 2 + f);
      },
      [&](int i, int f, bf16x8 wf) {
        if (f == 0) ak = MFMA(wf, as_bf(xf[i]), ak); else av = MFMA(as_bf(xf[i]), wf, av);
      },
      [&](int i) {
        if (i == 15 || i == KS_D) release(c, g0 + CH_KV + (i >> 3));
        x_next(rs, 2 * layer, T, lane, i, xf, stv);
        if (i == 26) var = finish_stats(stv, h, xf[KS_D]);
      });
  PIN(ak); PIN(av);
  const float rstd = __builtin_amdgcn_rsqf(var);
  const bool odd = head & 1;
  u32x4 kown[2], vown[2];
  pipe<KS_D + 1, 1, 4>(
      [&](int i, int) -> bf16x8 {
        if (i == 0) need(c, g0 + CH_Q + 1);
        return ldw(chunk(c, layer, CH_Q + (i >> 4)), i & 15);
      },
      [&](int i, int, bf16x8 wf) { aq = MFMA(wf, as_bf(xf[i]), aq); },
      [&](int i) { if (i == KS_D) release(c, g0 + CH_Q + 1); },
      [&]() {
        if (h == 0) rsl[t] = rstd;
#pragma unroll
        for (int r = 0; r < 16; ++r) ak[r] *= rstd;
#pragma unroll
        for (int g = 0; g < 4; ++g) {
          f32x4 rr = *(LAS f32x4*)(rsl + 8 * g + 4 * h);
#pragma unroll
          for (int r = 0; r < 4; ++r) av[4 * g + r] *= rr[r];
        }
        const bool one = t == (odd ? 0 : 28);
#pragma unroll
        for (int r = 0; r < 16; ++r) av[r] = one ? 1.f : av[r];
        kown[0] = pack8(ak, 0); kown[1] = pack8(ak, 1); vown[0] = pack8(av, 0); vown[1] = pack8(av, 1);
        const unsigned kvo = layer * SZ_KV + (head * NTT + T) * 2048;
        st16_sc1(rs, lane * 16, WS_K + kvo, kown[0]); st16_sc1(rs, lane * 16 + 1024, WS_K + kvo, kown[1]);
        st16_sc1(rs, lane * 16, WS_V + kvo, vown[0]); st16_sc1(rs, lane * 16 + 1024, WS_V + kvo, vown[1]);
      });
  PIN(aq);
  publish(p.ws, FL_KV + (layer * NH + head) * NTT + T, c.tag, lane);
  const float sc = rstd * QSCALE;
#pragma unroll
  for (int r = 0; r < 16; ++r) aq[r] *= sc;
  const u32x4 qf[2] = {pack8(aq, 0), pack8(aq, 1)};
  Flags kvf;
  { const int kt = T - 8 + (lane & 7);
    kvf.f = (GAS u64*)(p.ws + WS_FLAGS) + (size_t)(FL_KV + (layer * NH + head) * NTT + (kt < 0 ? T : kt)) * 16; kvf.tag = c.tag; kvf.mask = 0; }
  const unsigned hb = layer * SZ_KV + head * NTT * 2048;
  constexpr float NEG = -1e30f;
  int ln2 = c.lane; OPQ_V(ln2);
  const int vv = (ln2 & 31) - 4 * (ln2 >> 5);
  const int jlo = T < 8 ? 8 - T : 0;
  f32x16 o = {};
  auto attend = [&](auto edge_c) {
    constexpr bool EDGE = decltype(edge_c)::value;
    u32x4 kf[9][2], vf[9][2];
#pragma unroll
    for (int jj = 0; jj < 8; ++jj) {
      int kt = T - 8 + jj; if (EDGE) kt = kt < 0 ? 0 : kt;
      kvf.ready(jj);
#pragma unroll
      for (int s = 0; s < 2; ++s) kf[jj][s] = ld16_sc1(rs, lane * 16 + s * 1024, WS_K + hb + kt * 2048);
    }
    CBAR(); SCHED0();
    auto load_v = [&](int j0) {
#pragma unroll
      for (int jj = j0; jj < j0 + 4; ++jj) {
        int kt = T - 8 + jj; if (EDGE) kt = kt < 0 ? 0 : kt;
#pragma unroll
        for (int s = 0; s < 2; ++s) vf[jj][s] = ld16_sc1(rs, lane * 16 + s * 1024, WS_V + hb + kt * 2048);
      }
    };
    kf[8][0] = kown[0]; kf[8][1] = kown[1]; vf[8][0] = vown[0]; vf[8][1] = vown[1];
    auto tile_of = [](int q) { return q == 0 ? 8 : q - 1; };
    auto scores = [&](int jj, const f32x16& cin) -> f32x16 {
      f32x16 z = MFMA(as_bf(kf[jj][0]), as_bf(qf[0]), cin);
      return MFMA(as_bf(kf[jj][1]), as_bf(qf[1]), z);
    };
    const f32x16 zero = {};
    float m0 = NEG, m1 = NEG;
    f32x16 zb[2];
    zb[0] = scores(8, zero);
#pragma unroll
    for (int q = 0; q < 9; ++q) {
      const int jj = tile_of(q);
      if (q == 3) load_v(0);
      if (q + 1 < 9) zb[(q + 1) & 1] = scores(tile_of(q + 1), zero);
      SCHED0();
      f32x16& z = zb[q & 1];
      if (jj == 8 || jj == 0) {
#pragma unroll
        for (int r = 0; r < 16; ++r) {
          const bool le = ((r & 3) + 8 * (r >> 2)) <= vv;
          z[r] = (le == (jj == 8)) ? z[r] : NEG;
        }
      }
      float t0 = fmaxf(fmaxf(z[0], z[1]), z[2]), t1 = fmaxf(fmaxf(z[3], z[4]), z[5]);
      float t2 = fmaxf(fmaxf(z[6], z[7]), z[8]), t3 = fmaxf(fmaxf(z[9], z[10]), z[11]);
      t0 = fmaxf(fmaxf(t0, z[12]), z[13]); t1 = fmaxf(fmaxf(t1, z[14]), z[15]);
      if (EDGE && jj < jlo) { t0 = NEG; t1 = NEG; t2 = NEG; t3 = NEG; }
      m0 = fmaxf(fmaxf(m0, t0), t2); m1 = fmaxf(fmaxf(m1, t1), t3);
      PIN(m0); PIN(m1);
      SCHED0();
    }
    const float m = xhalf_max(fmaxf(m0, m1));
    f32x16 negm;
#pragma unroll
    for (int r = 0; r < 16; ++r) negm[r] = -m;
    zb[0] = scores(8, negm);
    sfor<0, 9>([&](auto qc) {
      constexpr int q = decltype(qc)::value, jj = q == 0 ? 8 : q - 1, jn = q;
      f32x16& z = zb[q & 1];
      f32x16& zn = zb[(q + 1) & 1];
      auto ex4 = [&](int r0) {
#pragma unroll
        for (int r = r0; r < r0 + 4; ++r) {
          float e = __builtin_amdgcn_exp2f(z[r]);
          if (jj == 8 || jj == 0) { const bool le = ((r & 3) + 8 * (r >> 2)) <= vv; e = (le == (jj == 8)) ? e : 0.f; }
          PIN(e); z[r] = e;
        }
      };
      SCHED0();
      if (q == 2) load_v(4);
      if (q + 1 < 9) zn = MFMA(as_bf(kf[jn][0]), as_bf(qf[0]), negm);
      SCHED0();
      ex4(0);
      SCHED0();
      if (q + 1 < 9) zn = MFMA(as_bf(kf[jn][1]), as_bf(qf[1]), zn);
      SCHED0();
      ex4(4);
      u32x4 p0 = pack8(z, 0);
      if (EDGE && jj < jlo) p0 = u32x4{0u, 0u, 0u, 0u};
      ex4(8);
      SCHED0();
      o = MFMA(as_bf(vf[jj][0]), as_bf(p0), o);
      SCHED0();
      ex4(12);
      u32x4 p1 = pack8(z, 1);
      if (EDGE && jj < jlo) p1 = u32x4{0u, 0u, 0u, 0u};
      SCHED0();
      o = MFMA(as_bf(vf[jj][1]), as_bf(p1), o);
    });
    SCHED0();
  };
  if (T >= 8) attend(BoolC<false>{}); else attend(BoolC<true>{});
  float l = odd ? o[0] : o[12];
  {
    auto r = __builtin_amdgcn_permlane32_swap(__float_as_uint(l), __float_as_uint(l), false, false);
    l = __uint_as_float(odd ? r[0] : r[1]);
  }
  const float inv = __builtin_amdgcn_rcpf(l);
#pragma unroll
  for (int r = 0; r < 16; ++r) o[r] *= inv;
  const unsigned oo = WS_O + layer * SZ_O + (T * KS_O + head * 2) * 1024;
  st16_sc1(rs, lane * 16, oo, pack8(o, 0));
  st16_sc1(rs, lane * 16 + 1024, oo, pack8(o, 1));
  publish(p.ws, FL_O + (layer * NTT + T) * 16 + head, c.tag, lane);
}

DI void phase_t2(const Params& p, CW& c, int layer, __amdgpu_buffer_rsrc_t rs) {
  PHASE_IDS();
  const unsigned g0 = layer * NCH;
  if (cj >= 14) { release(c, g0 + CH_WO + 3); return; }
  const int tp = cj & 1, ns = cj >> 1, tt = w & 1, kh = w >> 1, T = grp * 4 + tp * 2 + tt, dt = ns * 2 + kh;
  if (layer == 0) wait_flags(p.ws, FL_P + 223 + T, 1, c.tag, lane);
  u32x4 res[2];
  res[0] = ld16_sc1(rs, lane * 16, WS_X + 2 * layer * SZ_X + (T * KS_D + dt * 2) * 1024);
  res[1] = ld16_sc1(rs, lane * 16 + 1024, WS_X + 2 * layer * SZ_X + (T * KS_D + dt * 2) * 1024);
  u32x4 of[16];
  auto ld_o = [&](int i) -> u32x4 { return ld16_sc1(rs, lane * 16 + (i & 3) * 1024, WS_O + layer * SZ_O + (T * KS_O + kh * 16 + (i & ~3)) * 1024); };
  {
    Flags fl; fl.init(p.ws, FL_O + (layer * NTT + T) * 16 + kh * 8, 8, c.tag, lane);
    fl.ready_all(8);
#pragma unroll
    for (int i = 0; i < 8; ++i) of[i] = ld_o(i);
    SCHED0();
  }
  f32x16 acc[2] = {};
  pipe<16, 2, PF2>(
      [&](int i, int d2) -> bf16x8 {
        if (i == 0 && d2 == 0) need(c, g0 + CH_WO + 3);
        return ldw(chunk(c, layer, CH_WO + (i >> 2)) + kh * 8192, (i & 3) * 2 + d2);
      },
      [&](int i, int d2, bf16x8 wf) { acc[d2] = MFMA(wf, as_bf(of[i]), acc[d2]); },
      [&](int i) { if (i + 8 < 16) of[i + 8] = ld_o(i + 8); if (i == 15) release(c, g0 + CH_WO + 3); });
  PIN(acc[0]); PIN(acc[1]);
  f32x16 keep = pair_exchange(c, acc, kh);
  add_res(keep, res);
  res[0] = pack8(keep, 0); res[1] = pack8(keep, 1);
  store_x_tile(res, rs, 2 * layer + 1, T, dt, lane);
  publish(p.ws, FL_X + ((2 * layer + 1) * NTT + T) * 16 + dt, c.tag, lane);
}

DI float swiglu1(float g, float u, float c1, float var) {
  const float e = __builtin_amdgcn_exp2f(g * c1);
  const float rc = __builtin_amdgcn_rcpf(fmaf(e, var, var));
  return (g * u) * rc;
}
DI bool flags_set(u64 v, u64 tag) { return __all(v == tag); }
DI void finish_poll(u64 v, GAS u64* f, u64 tag) {
#pragma unroll 1
  for (unsigned spins = 0; spins < SPIN_CAP && !flags_set(v, tag); ++spins) v = __hip_atomic_load(f, RLX_AGENT);
  CBAR();
}

DI void phase_t34(const Params& p, CW& c, int layer, __amdgpu_buffer_rsrc_t rs) {
  PHASE_IDS();
  const int T = grp * 4 + w;
  const unsigned g0 = layer * NCH;
  const bool tile = cj < 14;
  const int tp = cj & 1, ns = cj >> 1, tt = w & 1, kh = w >> 1, T4 = grp * 4 + tp * 2 + tt, dt = ns * 2 + kh;
  GAS u64* hfl = (GAS u64*)(p.ws + WS_FLAGS) + (size_t)(FL_H + (layer * 3 * NTT + T4) * 16 + kh * 8 + (lane & 7)) * 16;
  const unsigned hfo = FL_H + (layer * 3 * NTT + T) * 16 + cj;
  u64 pv = 0;
  f32x16 a3[2] = {};
  float var = 0.f, c1 = 0.f;
  const unsigned hbase = WS_H + layer * SZ_H + (T * KS_F + cj * 7) * 1024;
  auto swi_store = [&](const f32x16& a, int tile) {
    u32x4 hp;
#pragma unroll
    for (int r = 0; r < 8; r += 2)
      hp[r >> 1] = pk2(swiglu1(a[r], a[8 + r], c1, var), swiglu1(a[r + 1], a[9 + r], c1, var));
    st16_sc1(rs, lane * 16 + tile * 1024, hbase, hp);
  };
  {
    u32x4 xf[KS_D + 1];
    u32x2 stv[7];
    x_first(p.ws, c.tag, rs, 2 * layer + 1, T, lane, xf);
    f32x16 a1[3] = {};
    pipe<KS_D + 1, 3, 2>(
        [&](int i, int f) -> bf16x8 {
          if (i % 10 == 0 && f == 0) need(c, g0 + CH_A + i / 5 + 1);
          return ldw(chunk(c, layer, CH_A + i / 5), (i % 5) * 3 + f);
        },
        [&](int i, int f, bf16x8 wf) { a1[f] = MFMA(wf, as_bf(xf[i]), a1[f]); },
        [&](int i) { if (i % 10 == 9 || i == KS_D) release(c, g0 + CH_A + i / 5); x_next(rs, 2 * layer + 1, T, lane, i, xf, stv); if (i == 26) var = finish_stats(stv, h, xf[KS_D]); });
    PIN(a1[0]); PIN(a1[1]); PIN(a1[2]);
    c1 = -__builtin_amdgcn_rsqf(var) * LOG2E;
    f32x16 a2[2] = {};
    pipe<KS_D + 1, 2, PF2>(
        [&](int i, int f) -> bf16x8 {
          if ((i & 15) == 0 && f == 0) need(c, g0 + CH_B + (i >> 3) + 1);
          return ldw(chunk(c, layer, CH_B + (i >> 3)), (i & 7) * 2 + f);
        },
        [&](int i, int f, bf16x8 wf) { a2[f] = MFMA(wf, as_bf(xf[i]), a2[f]); },
        [&](int i) {
          if (i == 16) publish(p.ws, hfo, c.tag, lane);
          if (i == 15 || i == KS_D) release(c, g0 + CH_B + (i >> 3));
        },
        [&]() { swi_store(a1[0], 0); swi_store(a1[1], 1); swi_store(a1[2], 2); });
    PIN(a2[0]); PIN(a2[1]);
    pipe<KS_D + 1, 2, PF2>(
        [&](int i, int f) -> bf16x8 {
          if ((i & 15) == 0 && f == 0) need(c, g0 + CH_E + (i >> 3) + 1);
          return ldw(chunk(c, layer, CH_E + (i >> 3)), (i & 7) * 2 + f);
        },
        [&](int i, int f, bf16x8 wf) { a3[f] = MFMA(wf, as_bf(xf[i]), a3[f]); },
        [&](int i) {
          if (i == 16) publish(p.ws, hfo + NTT * 16, c.tag, lane);
          if (i == 22) pv = __hip_atomic_load(hfl, RLX_AGENT);
          if (i == 15 || i == KS_D) release(c, g0 + CH_E + (i >> 3));
        },
        [&]() { swi_store(a2[0], 3); swi_store(a2[1], 4); });
    PIN(a3[0]); PIN(a3[1]);
  }
  if (!tile) {
    swi_store(a3[0], 5); swi_store(a3[1], 6);
    publish(p.ws, hfo + 2 * NTT * 16, c.tag, lane);
    release(c, g0 + NCH - 1);
    return;
  }
  const unsigned hb = WS_H + layer * SZ_H + (T4 * KS_F + kh * 56) * 1024;
  f32x16 acc[2] = {};
  u32x4 hf[56], res[2];
  auto ld_h = [&](int st) -> u32x4 {
    const int sl = st < 24 ? st / 3 : st < 40 ? (st - 24) / 2 : (st - 40) / 2, tl = st < 24 ? st % 3 : st < 40 ? 3 + (st & 1) : 5 + (st & 1);
    return ld16_sc1(rs, lane * 16 + (tl & 3) * 1024, hb + (sl * 7 + (tl & 4)) * 1024);
  };
  auto ldc = [&](int st, int d2) -> bf16x8 {
    if ((st & 7) == 0 && d2 == 0) need(c, g0 + CH_C + (st >> 2) + 1);
    return ldw(chunk(c, layer, CH_C + (st >> 2)) + kh * 8192, (st & 3) * 2 + d2);
  };
  auto relc = [&](int st) { if ((st & 7) == 7) release(c, g0 + CH_C + (st >> 2)); };
  pipe<56, 2, PF2>(
      ldc,
      [&](int st, int d2, bf16x8 wf) { acc[d2] = MFMA(wf, as_bf(hf[st]), acc[d2]); },
      [&](int st) {
        relc(st);
        if (st == 4) pv = __hip_atomic_load(hfl + NTT * 16 * 16, RLX_AGENT);
        if (st + HAHEAD == 24) finish_poll(pv, hfl + NTT * 16 * 16, c.tag);
        if (st == 20) pv = __hip_atomic_load(hfl + 2 * NTT * 16 * 16, RLX_AGENT);
        if (st + HAHEAD == 40) finish_poll(pv, hfl + 2 * NTT * 16 * 16, c.tag);
        if (st + HAHEAD < 56) hf[st + HAHEAD] = ld_h(st + HAHEAD);
        if (st == 46) {
          res[0] = ld16_sc1(rs, lane * 16, WS_X + (2 * layer + 1) * SZ_X + (T4 * KS_D + dt * 2) * 1024);
          res[1] = ld16_sc1(rs, lane * 16 + 1024, WS_X + (2 * layer + 1) * SZ_X + (T4 * KS_D + dt * 2) * 1024);
        }
      },
      [&]() {
        swi_store(a3[0], 5); swi_store(a3[1], 6);
        finish_poll(pv, hfl, c.tag);
#pragma unroll
        for (int i = 0; i < HAHEAD; ++i) hf[i] = ld_h(i);
        asm volatile("s_waitcnt vmcnt(%0)" ::"i"(HAHEAD) : "memory");
        set_flag(p.ws, hfo + 2 * NTT * 16, c.tag, lane);
      });
  PIN(acc[0]); PIN(acc[1]);
  f32x16 keep = pair_exchange(c, acc, kh);
  add_res(keep, res);
  res[0] = pack8(keep, 0); res[1] = pack8(keep, 1);
  if (layer == 0) {
    store_x_tile(res, rs, 2, T4, dt, lane);
    publish(p.ws, FL_X + (2 * NTT + T4) * 16 + dt, c.tag, lane);
  } else {
    char* rowp = p.out + ((size_t)(T4 * 32 + t) * D + dt * 32 + 4 * h) * 2;
    *(u32x2*)(rowp + 0) = u32x2{res[0][0], res[0][1]};
    *(u32x2*)(rowp + 16) = u32x2{res[0][2], res[0][3]};
    *(u32x2*)(rowp + 32) = u32x2{res[1][0], res[1][1]};
    *(u32x2*)(rowp + 48) = u32x2{res[1][2], res[1][3]};
  }
}

__global__ __launch_bounds__(512, 2) void mega(Params p) {
  extern __shared__ __attribute__((aligned(16))) char smem[];
  const int b = blockIdx.x, xcd = b & 7, idx = b >> 3;
  const int tid = threadIdx.x, lane = tid & 63, w = RFL(tid >> 6);
  const unsigned ldsbase = (unsigned)(size_t)smem;
  const int grp = xcd * 2 + (idx & 1), j = idx >> 1;
  if (tid < 16) ((LAS unsigned*)(smem + LDS_MISC))[tid] = 0u;
  u64 tag = __hip_atomic_load((GAS u64*)(p.ws + WS_EPOCH), RLX_AGENT);
  unsigned fltouch = 0;
  if (w == 7) { const unsigned ln = b * 64 + lane; fltouch = __hip_atomic_load((GAS unsigned*)(p.ws + WS_FLAGS + (size_t)(ln < FL_NUM ? ln : 0) * 128), RLX_AGENT); }
  {
    const int team = w >> 2, task = team == 0 ? (b < 223 ? b : 300) : (b >= 160 && b < 224 ? 223 + b - 160 : 300);
    const __amdgpu_buffer_rsrc_t rw = mkrsrc(p.ws, WS_FLAGS);
    LAS float* red = (LAS float*)(smem + LDS_EXCH + team * 1024);
    setup_part1(p, rw, task, w & 3, lane, (LAS char*)smem + w * 8192, red);
    asm volatile("s_waitcnt lgkmcnt(0)\n\ts_barrier" ::: "memory");
    setup_part2(rw, task, w & 3, lane, red);
    asm volatile("s_waitcnt vmcnt(0)\n\ts_barrier" ::: "memory");
    asm volatile("" : "+v"(tag));
    tag += 0x9E3779B97F4A7C15ull;
    asm volatile("" ::"v"(fltouch));
    if ((w & 3) == 0 && task < 287) set_flag(p.ws, FL_P + task, tag, lane);
    if (lane == 0) __hip_atomic_store((GAS u64*)(p.ws + WS_ARRIVE) + b * 8 + w, tag, RLX_AGENT);
  }
  if (w >= 4) { loader_main(p, w - 4, lane, j, ldsbase, tag); return; }
  CW c;
  c.sm = (LAS char*)smem; c.ldsbase = ldsbase; c.lane = lane; c.w = w; c.j = j; c.grp = grp;
  c.have = 0; c.exn = 0; c.tag = tag; c.pk = u32x4{0u, 0u, 0u, 0u};
  const __amdgpu_buffer_rsrc_t rs = mkrsrc(p.ws, WS_DATA_END);
  for (int layer_i = 0; layer_i < 2; ++layer_i) {
    int layer = layer_i; OPQ_S(layer);
    phase_t1(p, c, layer, rs);
    phase_t2(p, c, layer, rs);
    phase_t34(p, c, layer, rs);
  }
}

extern "C" void kernel_launch(void* const* d_in, const int* in_sizes, int n_in, void* d_out, int out_size, void* d_ws,
                              size_t ws_size, hipStream_t stream) {
  Params p;
  p.x = (const char*)d_in[0]; p.wqkv = (const char*)d_in[1]; p.wo = (const char*)d_in[2];
  p.w1 = (const char*)d_in[4]; p.w3 = (const char*)d_in[5]; p.w2 = (const char*)d_in[6];
  p.ws = (char*)d_ws; p.out = (char*)d_out;
  (void)hipFuncSetAttribute((const void*)mega, hipFuncAttributeMaxDynamicSharedMemorySize, LDS_TOTAL);
  mega<<<256, 512, LDS_TOTAL, stream>>>(p);
}
```
